```python
import torch

def _softmax(x, axis=-1):
    return torch.softmax(x, dim=axis)
def _gelu(x):
    return torch.nn.functional.gelu(x, approximate="tanh")
def _silu(x):
    return torch.nn.functional.silu(x)
def _sigmoid(x):
    return torch.sigmoid(x)
def _layernorm(x, axis=-1, eps=1e-5):
    m = x.mean(dim=axis, keepdim=True)
    v = x.var(dim=axis, keepdim=True, unbiased=False)
    return (x - m) / torch.sqrt(v + eps)
def _scatter_add(tgt, idx, src, axis=0):
    return tgt.clone().index_add_(axis, idx, src)
def _segment_sum(src, seg, n):
    out = torch.zeros((n,) + src.shape[1:], dtype=src.dtype)
    return out.index_add_(0, seg, src)
def _pad(x, pw):
    flat = tuple(v for pair in reversed(pw) for v in pair)
    return torch.nn.functional.pad(x, flat)

kernel_name = 'sequential_preLN__ssm_conv_s4__glu_gated_reglu__prompt'

def setup_inputs():

    _w = (lambda a: (lambda t: t.to(torch.bfloat16) if t.is_floating_point() else t)(torch.as_tensor(a)))
    def _A_setup():
        B, L, D, N = 1, 2048, 448, 256
        A_re = -(lambda _x: torch.exp(torch.as_tensor(_x)))((lambda *s: torch.randn(*s, dtype=torch.bfloat16) * 3.2)(D, N))
        A_im = torch.pi * (lambda *s: torch.randn(*s, dtype=torch.bfloat16) * 0.05)(D, N)
        B_re = (lambda *s: torch.randn(*s, dtype=torch.bfloat16) * 0.05)(D, N)
        B_im = (lambda *s: torch.randn(*s, dtype=torch.bfloat16) * 0.05)(D, N)
        C_re = (lambda *s: torch.randn(*s, dtype=torch.bfloat16) * 0.05)(D, N)
        C_im = (lambda *s: torch.randn(*s, dtype=torch.bfloat16) * 0.05)(D, N)
        log_dt = (lambda *s: torch.randn(*s, dtype=torch.bfloat16) * 0.05)(D) * 0.5 - 4.0
        Dskip = (lambda *s: torch.randn(*s, dtype=torch.bfloat16) * 0.05)(D)
        return {
            "u": _w((lambda *s: torch.randn(*s, dtype=torch.bfloat16) * 0.05)(B, L, D)),
            "A_re": _w(A_re),
            "A_im": _w(A_im),
            "B_re": _w(B_re),
            "B_im": _w(B_im),
            "C_re": _w(C_re),
            "C_im": _w(C_im),
            "log_dt": _w(log_dt),
            "Dskip": _w(Dskip),
        }
    _A_in = _A_setup()

    def _B_setup():
        B, S, D, F = 1, 2048, 448, 1792
        return {
            "x": _w((lambda *s: torch.randn(*s, dtype=torch.bfloat16) * 0.05)(B, S, D)),
            "W_gate": _w((lambda *s: torch.randn(*s, dtype=torch.bfloat16) * 0.4)(D, F) * (2.0 / D) ** 0.5),
            "W_up": _w((lambda *s: torch.randn(*s, dtype=torch.bfloat16) * 0.4)(D, F) * (2.0 / D) ** 0.5),
            "W_down": _w((lambda *s: torch.randn(*s, dtype=torch.bfloat16) * 0.4)(F, D) * (1.0 / F) ** 0.5),
        }
    _B_in = _B_setup()

    return {
        "A_u": _A_in["u"],
        "A_A_re": _A_in["A_re"],
        "A_A_im": _A_in["A_im"],
        "A_B_re": _A_in["B_re"],
        "A_B_im": _A_in["B_im"],
        "A_C_re": _A_in["C_re"],
        "A_C_im": _A_in["C_im"],
        "A_log_dt": _A_in["log_dt"],
        "A_Dskip": _A_in["Dskip"],
        "B_x": _B_in["x"],
        "B_W_gate": _B_in["W_gate"],
        "B_W_up": _B_in["W_up"],
        "B_W_down": _B_in["W_down"],
    }

def reference(A_u, A_A_re, A_A_im, A_B_re, A_B_im, A_C_re, A_C_im, A_log_dt, A_Dskip, B_x, B_W_gate, B_W_up, B_W_down):
    def _A_ref(u, A_re, A_im, B_re, B_im, C_re, C_im, log_dt, Dskip):
        Bsz, L, D = u.shape
        dt = torch.exp(log_dt)[:, None]
        A = A_re + 1j * A_im
        Bc = B_re + 1j * B_im
        Cc = C_re + 1j * C_im
        logdA = dt * A
        dB = dt * Bc
        l = (lambda x, dt: x.to(dt))(torch.arange(L), A_re.dtype)
        P = torch.exp(logdA[:, :, None] * l[None, None, :])
        K = torch.einsum("dn,dnl,dn->dl", Cc, P, dB).real
        L2 = 2 * L
        Kp = _pad(K, ((0, 0), (0, L)))
        up = (lambda x, *d: x.permute(*d))(u, 0, 2, 1)
        up = _pad(up, ((0, 0), (0, 0), (0, L)))
        Kf = (lambda x, axes=(-2,-1): torch.fft.rfft2(x.float(), dim=axes))(Kp[:, None, :], axes=(-2, -1))
        Uf = (lambda x, axes=(-2,-1): torch.fft.rfft2(x.float(), dim=axes))(up[:, :, None, :], axes=(-2, -1))
        Yf = Uf * Kf[None, :, :, :]
        y = (lambda x, s, axes=(-2,-1): torch.fft.irfft2(x, s=s, dim=axes).to(torch.bfloat16))(Yf, (1, L2), axes=(-2, -1))[:, :, 0, :L]
        y = (lambda x, *d: x.permute(*d))(y, 0, 2, 1) + u * Dskip[None, None, :]
        return y

    def _B_ref(x, W_gate, W_up, W_down):
        gate = torch.einsum("bsd,df->bsf", x, W_gate)
        up = torch.einsum("bsd,df->bsf", x, W_up)
        gate = gate * (lambda x, dt: x.to(dt))(gate > 0, gate.dtype)
        h = gate * up
        return torch.einsum("bsf,fd->bsd", h, W_down)

    x = A_u
    h = x + _A_ref(_layernorm(x, axis=-1), A_A_re, A_A_im, A_B_re, A_B_im, A_C_re, A_C_im, A_log_dt, A_Dskip)
    h2 = h + _B_ref(_layernorm(h, axis=-1), B_W_gate, B_W_up, B_W_down)
    return h2
```

```cpp
#include <hip/hip_runtime.h>
#include <hip/hip_bf16.h>
#include <stdint.h>

#define SPLIT_MODE 0
#define STAMPS 0

typedef unsigned short u16;
typedef unsigned int u32;
typedef unsigned long long u64;
typedef __attribute__((ext_vector_type(2))) float f32x2;
typedef __attribute__((ext_vector_type(4))) float f32x4;
typedef __attribute__((ext_vector_type(16))) float f32x16;
typedef __attribute__((ext_vector_type(2))) __bf16 bf16x2;
typedef __attribute__((ext_vector_type(8))) __bf16 bf16x8;
typedef __attribute__((ext_vector_type(4))) short s16x4;
typedef __attribute__((ext_vector_type(2))) u32 u32x2;
typedef __attribute__((ext_vector_type(4))) u32 u32x4;
#define LAS __attribute__((address_space(3)))
#define GAS __attribute__((address_space(1)))
#define DI __device__ __forceinline__

constexpr int SEQ = 2048, DM = 448, NST = 256, FF = 1792;
constexpr u32 SENT = 0xFFFFFFFFu;
constexpr int AUX_SC1 = 16;
constexpr u32 SPIN_CAP = 1u << 14;

constexpr int WS_EPOCH = 0;
constexpr int WS_STATS = 4096;
constexpr int WS_H2 = 32768;
constexpr int WS_H2_BYTES = SEQ * DM * 2;
constexpr int WS_HM = WS_H2 + WS_H2_BYTES + 4096 - (WS_H2 + WS_H2_BYTES) % 4096;
constexpr int WS_HM_BYTES = SEQ * FF * 2;
constexpr int WS_XP = WS_HM + WS_HM_BYTES;
constexpr int WS_DBG = WS_XP + 224 * SEQ * 8;
constexpr int WS_BYTES = WS_DBG + 65536;

constexpr int LDS_BYTES = 163840;
constexpr int PQ_ROW = 272, PQ_WAVE = 32 * PQ_ROW;
constexpr int L_U = 8 * PQ_WAVE;
constexpr int U_CH = (256 + SEQ) * 2;
constexpr int L_XS = L_U + 2 * U_CH, XS_ROW = 912;
constexpr int L_TAB = L_XS, TAB_CH = 648 * 4;
constexpr int L_XA = L_XS;
constexpr int L_DUMMY = L_XS + 8192;
constexpr int KC_COPY = 260 * 16, KC_CH = 8 * KC_COPY;
constexpr int W1_CHUNK = 32 * 448;
constexpr int W1_SLOTS = 11;
constexpr int L_CS = 0, L_ST = 1024;
constexpr int WD_SLOTS = 19;
static_assert(L_DUMMY + 256 <= LDS_BYTES - 5 * W1_CHUNK, "conv region overlaps early weight slots");
static_assert(2 * KC_CH <= L_U, "kernel copies");
static_assert(LDS_BYTES - W1_SLOTS * W1_CHUNK >= 2048, "small tables");

struct Params {
  const u16 *x, *are, *aim, *bre, *bim, *cre, *cim, *ldt, *dsk, *wg, *wu, *wd;
  u16* out;
  unsigned char* ws;
};

DI u32 pack_bf16(float lo, float hi) { f32x2 v = {lo, hi}; return __builtin_bit_cast(u32, __builtin_convertvector(v, bf16x2)); }
DI float bf_lo(u32 x) { return __uint_as_float(x << 16); }
DI float bf_hi(u32 x) { return __uint_as_float(x & 0xffff0000u); }
DI float bf1(u16 x) { return __uint_as_float((u32)x << 16); }
DI float rbf(float x) { return bf_lo(pack_bf16(x, x)); }
DI float dot2(u32 a, u32 b, float c) { return __builtin_amdgcn_fdot2_f32_bf16(__builtin_bit_cast(bf16x2, a), __builtin_bit_cast(bf16x2, b), c, false); }
DI f32x4 mfma16(u32x4 a, u32x4 b, f32x4 c) { return __builtin_amdgcn_mfma_f32_16x16x32_bf16(__builtin_bit_cast(bf16x8, a), __builtin_bit_cast(bf16x8, b), c, 0, 0, 0); }
DI f32x16 mfma32(u32x4 a, u32x4 b, f32x16 c) { return __builtin_amdgcn_mfma_f32_32x32x16_bf16(__builtin_bit_cast(bf16x8, a), __builtin_bit_cast(bf16x8, b), c, 0, 0, 0); }
DI u32x2 lds_tr(LAS unsigned char* p) { return __builtin_bit_cast(u32x2, __builtin_amdgcn_ds_read_tr16_b64_v4i16((LAS s16x4*)p)); }
DI void glds16(const void* gsrc, u32 lds_dst) {
  u32 keep;
  asm volatile("s_mov_b32 %0, m0\n\ts_mov_b32 m0, %2\n\ts_nop 0\n\tglobal_load_lds_dwordx4 %1, off\n\ts_mov_b32 m0, %0"
               : "=&s"(keep) : "v"(gsrc), "s"(lds_dst) : "memory");
}
DI void glds4(const void* gsrc, u32 lds_dst) {
  u32 keep;
  asm volatile("s_mov_b32 %0, m0\n\ts_mov_b32 m0, %2\n\ts_nop 0\n\tglobal_load_lds_dword %1, off\n\ts_mov_b32 m0, %0"
               : "=&s"(keep) : "v"(gsrc), "s"(lds_dst) : "memory");
}
#define VM_WAIT0() do { asm volatile("" ::: "memory"); __builtin_amdgcn_s_waitcnt(0x0F70); asm volatile("s_waitcnt vmcnt(0)" ::: "memory"); } while (0)
#define BARRIER() do { asm volatile("s_waitcnt lgkmcnt(0)" ::: "memory"); __builtin_amdgcn_s_barrier(); asm volatile("" ::: "memory"); } while (0)
#define WAVE_LDS_ORDER() asm volatile("" ::: "memory")

template <int CTRL> DI float dpp_add(float v) {
  return v + __builtin_bit_cast(float, __builtin_amdgcn_update_dpp(0, __builtin_bit_cast(int, v), CTRL, 0xf, 0xf, true));
}
DI float wave_sum(float v) {
  v = dpp_add<0xB1>(v);
  v = dpp_add<0x4E>(v);
  v = dpp_add<0x141>(v);
  v = dpp_add<0x140>(v);
  const int iv = __builtin_bit_cast(int, v);
  const float a = __builtin_bit_cast(float, __builtin_amdgcn_readlane(iv, 0)), b = __builtin_bit_cast(float, __builtin_amdgcn_readlane(iv, 16));
  const float c = __builtin_bit_cast(float, __builtin_amdgcn_readlane(iv, 32)), d = __builtin_bit_cast(float, __builtin_amdgcn_readlane(iv, 48));
  return (a + b) + (c + d);
}
DI float half_sum(float v) {
  const u32 x = __float_as_uint(v);
  auto r = __builtin_amdgcn_permlane32_swap(x, x, false, false);
  return __uint_as_float(r[0]) + __uint_as_float(r[1]);
}
typedef f32x2 cf;
struct cm { cf z, s; };
DI cm mk(cf z) { return {z, cf{-z[1], z[0]}}; }
DI cf cmul(cf a, const cm& b) { return __builtin_elementwise_fma(cf{a[0], a[0]}, b.z, cf{a[1], a[1]} * b.s); }
DI cf zpow(float a, float b, float l) {
  const float m = __builtin_amdgcn_exp2f(a * l);
  const float ph = __builtin_amdgcn_fractf(b * l);
  return cf{m * __builtin_amdgcn_cosf(ph), m * __builtin_amdgcn_sinf(ph)};
}
DI void ln_scalars(float mean, float var, float& m, float& s) {
  m = rbf(mean);
  s = rbf(__builtin_sqrtf(rbf(rbf(var) + 1e-5f)));
}
DI u32x4 tok_transpose(u32x4 v) {
  const auto a = __builtin_amdgcn_permlane16_swap(v[0], v[1], false, false);
  const auto b = __builtin_amdgcn_permlane16_swap(v[2], v[3], false, false);
  u32x4 o;
  o[0] = __builtin_amdgcn_update_dpp(a[0], b[0], 0x128, 0xf, 0xc, false);
  o[1] = __builtin_amdgcn_update_dpp(a[1], b[1], 0x128, 0xf, 0xc, false);
  o[2] = __builtin_amdgcn_update_dpp(b[0], a[0], 0x128, 0xf, 0x3, false);
  o[3] = __builtin_amdgcn_update_dpp(b[1], a[1], 0x128, 0xf, 0x3, false);
  return o;
}
DI int col_token(int j) { return 4 * (j & 7) + 2 * ((j >> 3) & 1) + ((j >> 4) & 1); }
DI u32 umax3(u32 a, u32 b, u32 c) { return max(max(a, b), c); }

#define STAMP(i) do {} while (0)
#define STAMPX(i) do {} while (0)
#define STAMPK(i) STAMPX(i)
#define STAMPG(i) do {} while (0)

template <int NT, bool STATS, bool SYNC_IN>
DI void gemm1_wave(const Params& p, LAS unsigned char* lds, __amdgpu_buffer_rsrc_t rs, int rb, int cb, int tg, int cg, int lane,
                   const unsigned char* wsrc, int w, bool sync_out) {
  const int h = lane >> 5;
  u32x4 tok[28];
  const int tvo = WS_H2 + rb * (224 * 512) + (4 * h + 2 * ((lane >> 3) & 1) + ((lane >> 4) & 1)) * 512 + (tg * 32 + 4 * (lane & 7)) * 4;
#define LOAD_TOK(s) tok[s] = __builtin_amdgcn_raw_buffer_load_b128(rs, tvo, (s) * 4096, SYNC_IN ? AUX_SC1 : 0)
#pragma unroll
  for (int s = 0; s < 21; ++s) LOAD_TOK(s);

  const int cgp = (lane >> 4) & 1, q = (lane & 15) >> 2, pp = lane & 3;
  const int lbase = (8 * h + q) * 448 + (pp >> 1) * 224 + 16 * cgp + 8 * (pp & 1);
  int tile[NT];
#pragma unroll
  for (int i = 0; i < NT; ++i) { int c = tg + i; if (NT == 4) c &= 3; else c = 4 + (c >= 3 ? c - 3 : c) - (c >= 6 ? 3 : 0); tile[i] = c; }
  LAS unsigned char* abase[NT][3];
#pragma unroll
  for (int i = 0; i < NT; ++i)
#pragma unroll
    for (int k = 0; k < 3; ++k) { u32 a = (u32)(size_t)lds + lbase + tile[i] * 32 + k * 57344; asm volatile("" : "+v"(a)); abase[i][k] = (LAS unsigned char*)(size_t)a; }
  f32x16 acc[NT];
#pragma unroll
  for (int i = 0; i < NT; ++i)
#pragma unroll
    for (int r = 0; r < 16; ++r) acc[i][r] = 0.f;
  float cs = 0.f, s1 = 0.f, s2 = 0.f;
  constexpr u32 ONES = 0x3F803F80u;

  u32x4 wf[2][NT];
#define LOAD_WF(s) do { const int cb_ = LDS_BYTES - ((((s) >> 1) % W1_SLOTS) + 1) * W1_CHUNK + ((s) & 1) * 7168; \
    _Pragma("unroll") for (int i = 0; i < NT; ++i) { LAS unsigned char* a_ = abase[i][cb_ / 57344] + cb_ % 57344; \
      const u32x2 lo_ = lds_tr(a_), hi_ = lds_tr(a_ + 4 * 448); wf[(s) & 1][i] = u32x4{lo_[0], lo_[1], hi_[0], hi_[1]}; } } while (0)
  __builtin_amdgcn_sched_barrier(0);
  LOAD_WF(0);
#pragma unroll
  for (int g = 0; g < 4; ++g) {
    if (SYNC_IN) {
      for (u32 spin = 0; spin < SPIN_CAP; ++spin) {
        u32 m = 0;
#pragma unroll
        for (int s = 7 * g; s < 7 * g + 7; ++s) m = umax3(umax3(m, tok[s][0], tok[s][1]), tok[s][2], tok[s][3]);
        if (!__any(m == SENT)) break;
        __builtin_amdgcn_s_sleep(2);
#pragma unroll
        for (int s = 7 * g; s < 7 * g + 7; ++s) LOAD_TOK(s);
      }
    }
#pragma unroll
    for (int s = 7 * g; s < 7 * g + 7; ++s) {
      if (s == 6) {
        BARRIER();
        if (w < 7) {
#pragma unroll
          for (int s2i = 22; s2i < 28; ++s2i)
            glds16(wsrc + (size_t)s2i * (16 * FF * 2), LDS_BYTES - (((s2i >> 1) % W1_SLOTS) + 1) * W1_CHUNK + (s2i & 1) * 7168 + w * 1024);
        }
      }
      if (s == 21) { VM_WAIT0(); BARRIER(); }
      if (s == 1) STAMPG(12);
      if (s == 7) STAMPG(13);
      if (s == 20) STAMPG(14);
      __builtin_amdgcn_sched_barrier(0);
      if (s + 1 < 28) LOAD_WF(s + 1);
      const u32x4 tf = tok_transpose(tok[s]);
      if (s < 7) LOAD_TOK(s + 21);
#pragma unroll
      for (int i = 0; i < NT; ++i) acc[i] = mfma32(wf[s & 1][i], tf, acc[i]);
#pragma unroll
      for (int e = 0; e < 4; ++e) cs = dot2(wf[s & 1][0][e], ONES, cs);
      if (STATS) {
#pragma unroll
        for (int e = 0; e < 4; ++e) { s1 = dot2(tf[e], ONES, s1); s2 = dot2(tf[e], tf[e], s2); }
      }
      asm volatile("" : "+v"(cs), "+v"(s1), "+v"(s2));
      __builtin_amdgcn_sched_barrier(0);
    }
  }
#undef LOAD_WF
  STAMPG(15);
#undef LOAD_TOK
  cs = half_sum(cs);
  if (lane < 32 && (NT == 4 || tg < 3)) *(LAS float*)(lds + L_CS + (tile[0] * 32 + lane) * 4) = cs;
  if (STATS) {
    s1 = half_sum(s1); s2 = half_sum(s2);
    const float mean = s1 * (1.f / DM);
    const float var = fmaxf(s2 * (1.f / DM) - mean * mean, 0.f);
    float m, sd; ln_scalars(mean, var, m, sd);
    if (lane < 32) *(LAS f32x2*)(lds + L_ST + (tg * 32 + lane) * 8) = f32x2{m, __builtin_amdgcn_rcpf(sd)};
  }
  BARRIER();
  const f32x2 st = *(LAS f32x2*)(lds + L_ST + (tg * 32 + (lane & 31)) * 8);
  const float nm = -st[0] * st[1];
#pragma unroll
  for (int i = 0; i < NT; ++i) {
    f32x4 c4[4];
#pragma unroll
    for (int k = 0; k < 4; ++k) c4[k] = *(LAS f32x4*)(lds + L_CS + (tile[i] * 32 + 8 * k + 4 * h) * 4);
    float v[16];
#pragma unroll
    for (int r = 0; r < 16; ++r) v[r] = acc[i][r] * st[1] + nm * c4[r >> 2][r & 3];
    u32x4 o;
#pragma unroll
    for (int k = 0; k < 2; ++k)
#pragma unroll
      for (int e = 0; e < 2; ++e) {
        const u32 gp = pack_bf16(v[8 * k + 2 * e], v[8 * k + 2 * e + 1]), up = pack_bf16(v[8 * k + 4 + 2 * e], v[8 * k + 4 + 2 * e + 1]);
        const float g0 = bf_lo(gp), g1 = bf_hi(gp);
        const float a0 = (g0 > 0.f ? g0 : g0 * 0.f) * bf_lo(up), a1 = (g1 > 0.f ? g1 : g1 * 0.f) * bf_hi(up);
        o[2 * k + e] = min(pack_bf16(a0, a1), SENT - 1);
      }
    const int off = WS_HM + (((rb * 4 + tg) * 112 + cb * 7 + tile[i]) * 64 + lane) * 16;
    if (sync_out) __builtin_amdgcn_raw_buffer_store_b128(o, rs, off, 0, AUX_SC1);
    else __builtin_amdgcn_raw_buffer_store_b128(o, rs, off, 0, 0);
  }
}

template <int PH>
__global__ void __launch_bounds__(512, 2) fused_kernel(Params p) {
  constexpr bool DO_A = PH & 1, DO_B = PH & 2, DO_C = PH & 4, DO_D = PH & 8;
  constexpr bool S_AB = DO_A && DO_B, S_BC = DO_B && DO_C, S_CD = DO_C && DO_D;
  __shared__ __attribute__((aligned(16))) unsigned char lds_raw[LDS_BYTES];
  LAS unsigned char* lds = (LAS unsigned char*)lds_raw;
  const int tid = threadIdx.x, lane = tid & 63, w = __builtin_amdgcn_readfirstlane(tid >> 6);
  const int b = blockIdx.x, xcd = b & 7, idx = b >> 3;
  const __amdgpu_buffer_rsrc_t rs = __builtin_amdgcn_make_buffer_rsrc(p.ws, 0, WS_BYTES, 0x00020000);
  const bool conv_wg = idx < 28;
  const int pair = xcd * 28 + idx;
  const int rb = 4 * (xcd >> 1) + (idx >> 3), cb = 8 * (xcd & 1) + (idx & 7);
  STAMP(DO_A ? 0 : DO_B ? 9 : DO_C ? 10 : 11);

  const unsigned char* wsrc;
  {
    const int pc = (w < 7 ? w : 0) * 64 + lane, row = pc / 28, c = pc % 28;
    wsrc = (const unsigned char*)(c < 14 ? p.wg : p.wu) + row * (FF * 2) + cb * 224 + (c < 14 ? c : c - 14) * 16;
  }

  u32 tag = 0;
  if (S_AB) tag = __builtin_amdgcn_raw_buffer_load_b32(rs, 0, WS_EPOCH, AUX_SC1) + 1;
  if (S_BC && tid < 448) __builtin_amdgcn_raw_buffer_store_b128(u32x4{SENT, SENT, SENT, SENT}, rs, WS_H2 + b * 7168 + tid * 16, 0, AUX_SC1);
  asm volatile("" ::: "memory");

  u32x4 xr = {0, 0, 0, 0};
  if (DO_A && lane < 56) xr = *(const GAS u32x4*)(p.x + (size_t)(8 * b + w) * DM + lane * 8);

  const int l0 = 256 * w + 16 * (lane & 15) + 4 * (lane >> 4);
  u16 pa[6], pld = 0, pd0 = 0, pd1 = 0;
  const int ch = w >> 2;
  if (DO_B && conv_wg) {
    const int o = (2 * pair + ch) * NST + (w & 3) * 64 + lane;
    pa[0] = p.are[o]; pa[1] = p.aim[o]; pa[2] = p.bre[o]; pa[3] = p.bim[o]; pa[4] = p.cre[o]; pa[5] = p.cim[o];
    pld = p.ldt[2 * pair + ch]; pd0 = p.dsk[2 * pair]; pd1 = p.dsk[2 * pair + 1];
  }
  if (DO_C && !DO_B && w < 7) {
#pragma unroll
    for (int s = 0; s < 22; ++s)
      glds16(wsrc + (size_t)s * (16 * FF * 2), LDS_BYTES - ((s >> 1) + 1) * W1_CHUNK + (s & 1) * 7168 + w * 1024);
  }

  if (DO_A) {
    if (lane < 56) *(LAS u32x4*)(lds + L_XS + w * XS_ROW + lane * 16) = xr;
    BARRIER();
    if (tid < 448) {
#pragma unroll
      for (int k = 0; k < 2; ++k) {
        const int e = tid + 448 * k, pr = e >> 2, tp = e & 3;
        const u32 x0 = *(LAS u32*)(lds + L_XS + (2 * tp) * XS_ROW + pr * 4), x1 = *(LAS u32*)(lds + L_XS + (2 * tp + 1) * XS_ROW + pr * 4);
        __builtin_amdgcn_raw_buffer_store_b128(u32x4{x0, tag, x1, tag}, rs, WS_XP + pr * (SEQ * 8) + (8 * b + 2 * tp) * 8, 0, S_AB ? AUX_SC1 : 0);
      }
    }
    float f[8];
#pragma unroll
    for (int e = 0; e < 4; ++e) { f[2 * e] = bf_lo(xr[e]); f[2 * e + 1] = bf_hi(xr[e]); }
    float s = ((f[0] + f[1]) + (f[2] + f[3])) + ((f[4] + f[5]) + (f[6] + f[7]));
    const float mean = wave_sum(s) * (1.f / DM);
    float q = 0.f;
#pragma unroll
    for (int e = 0; e < 8; ++e) { const float d = f[e] - mean; q += d * d; }
    if (lane >= 56) q = 0.f;
    const float var = wave_sum(q) * (1.f / DM);
    float m, sd; ln_scalars(mean, var, m, sd);
    if (lane == 0) {
      const u32x2 g = {pack_bf16(m, sd), tag};
      __builtin_amdgcn_raw_buffer_store_b64(g, rs, WS_STATS + (8 * b + w) * 8, 0, S_AB ? AUX_SC1 : 0);
    }
  }
  if (DO_A) STAMP(1);

  if (DO_B) {
    u32x4 af[16];
    if (conv_wg) asm volatile("" :: "v"(pa[0]), "v"(pa[1]), "v"(pa[2]), "v"(pa[3]), "v"(pa[4]), "v"(pa[5]), "v"(pld), "v"(pd0), "v"(pd1));
    if (DO_C && w >= 4) {
#pragma unroll
      for (int k = 0; k < 2; ++k) {
        const int m = (w - 4) + 4 * k;
        if (m < 7) {
          const int pc = m * 64 + lane, row = pc / 28, c = pc % 28;
          const unsigned char* src = (const unsigned char*)(c < 14 ? p.wg : p.wu) + row * (FF * 2) + cb * 224 + (c < 14 ? c : c - 14) * 16;
#pragma unroll
          for (int s = 0; s < 10; ++s)
            glds16(src + (size_t)s * (16 * FF * 2), LDS_BYTES - ((s >> 1) + 1) * W1_CHUNK + (s & 1) * 7168 + m * 1024);
        }
      }
      const int t = (idx * 4 + (w - 4)) * 64 + lane;
      {
        const int ln = t, mat = ln >= 4032 ? 1 : 0, r = ln - mat * 4032;
        if (ln < 8064) glds4((const unsigned char*)(mat ? p.wu : p.wg) + (160 + r / 14) * (FF * 2) + (xcd & 1) * 1792 + (r % 14) * 128, L_DUMMY);
      }
      if (DO_D) {
        glds4((const unsigned char*)p.wd + t * 128, L_DUMMY);
        if (t + 8192 < 12544) glds4((const unsigned char*)p.wd + (t + 8192) * 128, L_DUMMY);
      }
    }
    if (conv_wg) {
      if ((S_CD || SPLIT_MODE == 3) && w < 4) {
#pragma unroll
        for (int e = 0; e < 8; ++e)
          __builtin_amdgcn_raw_buffer_store_b128(u32x4{SENT, SENT, SENT, SENT}, rs, WS_HM + pair * 32768 + e * 4096 + tid * 16, 0, AUX_SC1);
      }
      if (tid < 64) *(LAS u32x4*)(lds + L_U + (tid >> 5) * U_CH + (tid & 31) * 16) = u32x4{0, 0, 0, 0};
      const float dt = rbf(__expf(bf1(pld)));
      const float a = dt * bf1(pa[0]) * 1.44269504088896341f, bq = dt * bf1(pa[1]) * 0.15915494309189535f;
      const cf c0 = cmul(cf{bf1(pa[4]), bf1(pa[5])}, mk(cf{dt * bf1(pa[2]), dt * bf1(pa[3])}));
      STAMPK(12);
      LAS unsigned char* pq = lds + w * PQ_WAVE;
      LAS unsigned char* pqw = pq + lane * 4;
      {
        const cf z8 = zpow(a, bq, 8.f);
        const cm z16 = mk(cmul(z8, mk(z8)));
        cf e0 = {1.f, 0.f}, e1 = z8;
#pragma unroll
        for (int k = 0; k < 16; ++k) {
          *(LAS u32*)(pqw + (2 * k) * PQ_ROW) = pack_bf16(e0[0], e0[1]);
          *(LAS u32*)(pqw + (2 * k + 1) * PQ_ROW) = pack_bf16(e1[0], e1[1]);
          if (k < 15) { e0 = k == 0 ? z16.z : cmul(e0, z16); e1 = cmul(e1, z16); }
        }
      }
      WAVE_LDS_ORDER();
      u32x4 qf[8];
      {
        LAS unsigned char* rd = pq + (lane & 31) * PQ_ROW + (lane >> 5) * 16;
#pragma unroll
        for (int s = 0; s < 8; ++s) qf[s] = *(LAS u32x4*)(rd + s * 32);
      }
      WAVE_LDS_ORDER();
      {
        const cm z1 = mk(zpow(a, bq, 1.f)), w1 = mk(zpow(a, bq, 256.f));
        const cm w2 = mk(cmul(w1.z, w1)), w3 = mk(cmul(w2.z, w1)), w4 = mk(cmul(w2.z, w2));
        cf pw[8];
        pw[0] = c0;
#pragma unroll
        for (int k = 1; k < 8; ++k) pw[k] = cmul(pw[k - 1], z1);
#define PUT_P(row, v) do { const cf t_ = (v); *(LAS u32*)(pqw + (row) * PQ_ROW) = pack_bf16(t_[0], -t_[1]); } while (0)
#pragma unroll
        for (int k = 0; k < 8; ++k) PUT_P(k, pw[k]);
#pragma unroll
        for (int k = 0; k < 4; ++k) PUT_P(8 + k, cmul(pw[2 * k], w1));
        PUT_P(12, cmul(pw[0], w2)); PUT_P(13, cmul(pw[4], w2)); PUT_P(14, cmul(pw[0], w3)); PUT_P(15, cmul(pw[4], w3));
        const cf c4 = cmul(pw[0], w4);
        PUT_P(16, c4); PUT_P(17, cmul(c4, w1)); PUT_P(18, cmul(c4, w2)); PUT_P(19, cmul(c4, w3)); PUT_P(20, cmul(c4, w4));
#undef PUT_P
      }
      WAVE_LDS_ORDER();
      STAMPK(13);
      f32x16 kd;
#pragma unroll
      for (int r = 0; r < 16; ++r) kd[r] = 0.f;
      {
        LAS unsigned char* rd = pq + (lane & 31) * PQ_ROW + (lane >> 5) * 16;
#pragma unroll
        for (int s = 0; s < 8; ++s) kd = mfma32(*(LAS u32x4*)(rd + s * 32), qf[s], kd);
      }
      WAVE_LDS_ORDER();
#pragma unroll
      for (int k = 0; k < 3; ++k) *(LAS f32x4*)(pq + (k * 64 + lane) * 16) = f32x4{kd[4 * k], kd[4 * k + 1], kd[4 * k + 2], kd[4 * k + 3]};
      BARRIER();
      STAMPK(14);
      const int t = tid & 255;
      LAS float* tab = (LAS float*)(lds + L_TAB + ch * TAB_CH);
      if (t < 192) {
        f32x4 s = *(LAS f32x4*)(lds + (ch * 4) * PQ_WAVE + t * 16);
#pragma unroll
        for (int k = 1; k < 4; ++k) s += *(LAS f32x4*)(lds + (ch * 4 + k) * PQ_WAVE + t * 16);
        const int rq = t >> 6, q = t & 31, h = (t >> 5) & 1;
#pragma unroll
        for (int e = 0; e < 4; ++e) {
          int ix;
          if (rq == 0) ix = 8 * q + 4 * h + e;
          else if (rq == 1) ix = h == 0 ? 256 + 4 * q + e : 384 + 64 * (e >> 1) + 2 * q + (e & 1);
          else ix = h == 0 ? 512 + 32 * e + q : ((e == 0 && q == 0) ? 640 : 644);
          tab[ix] = s[e];
        }
      }
      BARRIER();
      STAMPK(15);
      {
        float kv[15];
#pragma unroll
        for (int e = 0; e < 15; ++e) {
          const int l = 2047 - 8 * t - e;
          const int bits = (int)(pack_bf16((float)l, 0.f) & 0xffffu) - 17024;
          const float v = tab[l < 256 ? max(l, 0) : bits];
          kv[e] = l < 0 ? 0.f : v;
        }
        u32 ev[7], od[7];
#pragma unroll
        for (int m = 0; m < 7; ++m) { ev[m] = pack_bf16(kv[2 * m], kv[2 * m + 1]); od[m] = pack_bf16(kv[2 * m + 1], kv[2 * m + 2]); }
        LAS unsigned char* kc = lds + ch * KC_CH + t * 16;
#pragma unroll
        for (int sg = 0; sg < 4; ++sg) {
          *(LAS u32x4*)(kc + (2 * sg) * KC_COPY) = u32x4{ev[sg], ev[sg + 1], ev[sg + 2], ev[sg + 3]};
          *(LAS u32x4*)(kc + (2 * sg + 1) * KC_COPY) = u32x4{od[sg], od[sg + 1], od[sg + 2], od[sg + 3]};
        }
        if (t < 8) *(LAS u32x4*)(lds + ch * KC_CH + t * KC_COPY + 256 * 16) = u32x4{0, 0, 0, 0};
      }
      BARRIER();
      {
        const int r = lane & 15, g = lane >> 4;
        LAS unsigned char* kr = lds + ch * KC_CH + (7 - (r & 7)) * KC_COPY + (256 - 8 * (w & 3) - g - (r >> 3)) * 16;
#pragma unroll
        for (int c = 0; c < 2; ++c)
#pragma unroll
          for (int i = 0; i < 8; ++i) af[c * 8 + i] = *(LAS u32x4*)(kr - (4 * c + 32 * i) * 16);
      }
    }
    STAMP(2);
    if (w < 4) {
      u32x4 g[4], x[4];
      const int xo = WS_XP + (conv_wg ? pair : 0) * (SEQ * 8) + tid * 64;
      for (u32 spin = 0; spin < SPIN_CAP; ++spin) {
#pragma unroll
        for (int k = 0; k < 4; ++k) g[k] = __builtin_amdgcn_raw_buffer_load_b128(rs, WS_STATS + tid * 64 + k * 16, 0, S_AB ? AUX_SC1 : 0);
#pragma unroll
        for (int k = 0; k < 4; ++k) x[k] = __builtin_amdgcn_raw_buffer_load_b128(rs, xo + k * 16, 0, S_AB ? AUX_SC1 : 0);
        if (!S_AB) break;
        bool ok = true;
#pragma unroll
        for (int k = 0; k < 4; ++k) ok = ok && g[k][1] == tag && g[k][3] == tag && x[k][1] == tag && x[k][3] == tag;
        if (__all(ok)) break;
        __builtin_amdgcn_s_sleep(2);
      }
      STAMP(3);
      if (conv_wg) {
        u32 u[8];
#pragma unroll
        for (int e = 0; e < 8; ++e) {
          const u32 st = g[e >> 1][2 * (e & 1)], xv = x[e >> 1][2 * (e & 1)];
          const float m = bf_lo(st), ri = __builtin_amdgcn_rcpf(bf_hi(st));
          const u32 d = pack_bf16(bf_lo(xv) - m, bf_hi(xv) - m);
          u[e] = pack_bf16(bf_lo(d) * ri, bf_hi(d) * ri);
        }
        u32x4 u0, u1;
#pragma unroll
        for (int e = 0; e < 4; ++e) { u0[e] = __builtin_amdgcn_perm(u[2 * e + 1], u[2 * e], 0x05040100u); u1[e] = __builtin_amdgcn_perm(u[2 * e + 1], u[2 * e], 0x07060302u); }
        *(LAS u32x4*)(lds + L_U + (256 + 8 * tid) * 2) = u0;
        *(LAS u32x4*)(lds + L_U + U_CH + (256 + 8 * tid) * 2) = u1;
        *(LAS u32x4*)(lds + L_XA + tid * 32) = u32x4{x[0][0], x[0][2], x[1][0], x[1][2]};
        *(LAS u32x4*)(lds + L_XA + tid * 32 + 16) = u32x4{x[2][0], x[2][2], x[3][0], x[3][2]};
      }
      VM_WAIT0();
    }
    BARRIER();
    if (S_AB && tid == 0 && b == 0) __builtin_amdgcn_raw_buffer_store_b32(tag, rs, 0, WS_EPOCH, AUX_SC1);
    if (conv_wg) {
      f32x4 acc[8];
#pragma unroll
      for (int t = 0; t < 8; ++t) acc[t] = f32x4{0.f, 0.f, 0.f, 0.f};
      LAS unsigned char* ub = lds + L_U + ch * U_CH + (256 - 64 * (w & 3) + 16 * (lane & 15) + 8 - 8 * (lane >> 4)) * 2;
#pragma unroll
      for (int c = 0; c < 2; ++c)
#pragma unroll
        for (int j = 0; j < 8; ++j) {
          const u32x4 bfr = *(LAS u32x4*)(ub + (256 * j - 32 * c) * 2);
#pragma unroll
          for (int i = 0; i + j < 8; ++i) acc[i + j] = mfma16(af[c * 8 + i], bfr, acc[i + j]);
        }
#pragma unroll
      for (int t = 0; t < 8; ++t) *(LAS f32x4*)(lds + w * 8192 + (t * 64 + lane) * 16) = acc[t];
      BARRIER();
      u32x4 o;
      f32x4 y[2];
#pragma unroll
      for (int c = 0; c < 2; ++c) {
        y[c] = *(LAS f32x4*)(lds + (4 * c) * 8192 + tid * 16);
#pragma unroll
        for (int k = 1; k < 4; ++k) y[c] += *(LAS f32x4*)(lds + (4 * c + k) * 8192 + tid * 16);
      }
      const u32x4 xc = *(LAS u32x4*)(lds + L_XA + l0 * 4);
      const u32x2 ua = *(LAS u32x2*)(lds + L_U + (256 + l0) * 2), ub2 = *(LAS u32x2*)(lds + L_U + U_CH + (256 + l0) * 2);
#pragma unroll
      for (int e = 0; e < 4; ++e) {
        const float u0 = (e & 1) ? bf_hi(ua[e >> 1]) : bf_lo(ua[e >> 1]), u1 = (e & 1) ? bf_hi(ub2[e >> 1]) : bf_lo(ub2[e >> 1]);
        const u32 sk = pack_bf16(u0 * bf1(pd0), u1 * bf1(pd1));
        const u32 yb = pack_bf16(y[0][e], y[1][e]);
        const u32 ys = pack_bf16(bf_lo(yb) + bf_lo(sk), bf_hi(yb) + bf_hi(sk));
        o[e] = min(pack_bf16(bf_lo(xc[e]) + bf_lo(ys), bf_hi(xc[e]) + bf_hi(ys)), SENT - 1);
      }
      __builtin_amdgcn_raw_buffer_store_b128(o, rs, WS_H2 + ((l0 >> 7) * 224 + pair) * 512 + (l0 & 127) * 4, 0, S_BC ? AUX_SC1 : 0);
    }
    STAMP(4);
    if (DO_C) {
      BARRIER();
      if (w < 7) {
#pragma unroll
        for (int s = 10; s < 22; ++s)
          glds16(wsrc + (size_t)s * (16 * FF * 2), LDS_BYTES - ((s >> 1) + 1) * W1_CHUNK + (s & 1) * 7168 + w * 1024);
      }
    }
  }

  if (DO_C) {
    VM_WAIT0();
    BARRIER();
    STAMP(5);
    if (w < 4) gemm1_wave<4, false, S_BC>(p, lds, rs, rb, cb, w, 0, lane, wsrc, w, S_CD);
    else gemm1_wave<3, true, S_BC>(p, lds, rs, rb, cb, w - 4, 1, lane, wsrc, w, S_CD);
    STAMP(6);
  }

  if (DO_D && conv_wg) {
    const int rb2 = 4 * xcd + idx / 7, db = idx % 7;
    const int kq = w & 3, ts = w >> 2, h = lane >> 5;
    if (DO_C) BARRIER();
    const unsigned char* dsrc = (const unsigned char*)p.wd + (size_t)(kq * 448 + ts * 8 + (lane >> 3)) * (DM * 2) + db * 128 +
                                (((lane & 7) ^ (((lane >> 4) & 1) << 2)) * 16);
    const u32 dbase = kq * (WD_SLOTS * 2048) + ts * 1024;
#pragma unroll
    for (int s = 0; s < WD_SLOTS; ++s) glds16(dsrc + (size_t)s * (16 * DM * 2), dbase + s * 2048);
    u32x4 hf[28];
    const int hvo = WS_HM + (((rb2 * 2 + ts) * 112 + kq * 28) * 64 + lane) * 16;
#define LOAD_HM(s) hf[s] = __builtin_amdgcn_raw_buffer_load_b128(rs, hvo, (s) * 1024, S_CD ? AUX_SC1 : 0)
#pragma unroll
    for (int s = 0; s < 28; ++s) LOAD_HM(s);
    u32 hr[4];
    {
      const int tk = rb2 * 64 + (tid >> 3);
      const int o = WS_H2 + ((tk >> 7) * 224 + db * 32 + (tid & 7) * 4) * 512 + (tk & 127) * 4;
#pragma unroll
      for (int e = 0; e < 4; ++e) hr[e] = __builtin_amdgcn_raw_buffer_load_b32(rs, o + e * 512, 0, S_BC ? AUX_SC1 : 0);
    }
    asm volatile("s_waitcnt vmcnt(32)" ::: "memory");
    BARRIER();
    STAMP(7);
    const int cgp = (lane >> 4) & 1, q = (lane & 15) >> 2, pp = lane & 3;
    LAS unsigned char* ab[2];
#pragma unroll
    for (int ct = 0; ct < 2; ++ct) ab[ct] = lds + kq * (WD_SLOTS * 2048) + (4 * h + q) * 128 + ((ct ^ (q >> 1)) * 64) + 32 * cgp + 8 * pp;
    f32x16 acc[2];
#pragma unroll
    for (int ct = 0; ct < 2; ++ct)
#pragma unroll
      for (int r = 0; r < 16; ++r) acc[ct][r] = 0.f;
#pragma unroll
    for (int g = 0; g < 4; ++g) {
      if (S_CD) {
        for (u32 spin = 0; spin < SPIN_CAP; ++spin) {
          u32 m = 0;
#pragma unroll
          for (int s = 7 * g; s < 7 * g + 7; ++s) m = umax3(umax3(m, hf[s][0], hf[s][1]), hf[s][2], hf[s][3]);
          if (!__any(m == SENT)) break;
          __builtin_amdgcn_s_sleep(2);
#pragma unroll
          for (int s = 7 * g; s < 7 * g + 7; ++s) LOAD_HM(s);
        }
      }
#pragma unroll
      for (int s = 7 * g; s < 7 * g + 7; ++s) {
        if (s == 28 - WD_SLOTS) {
          BARRIER();
#pragma unroll
          for (int s2 = WD_SLOTS; s2 < 28; ++s2) glds16(dsrc + (size_t)s2 * (16 * DM * 2), dbase + (s2 - WD_SLOTS) * 2048);
        }
        if (s == WD_SLOTS) { VM_WAIT0(); BARRIER(); }
        const int so = (s % WD_SLOTS) * 2048;
#pragma unroll
        for (int ct = 0; ct < 2; ++ct) {
          const u32x2 lo = lds_tr(ab[ct] + so), hi = lds_tr(ab[ct] + so + 1024);
          acc[ct] = mfma32(u32x4{lo[0], lo[1], hi[0], hi[1]}, hf[s], acc[ct]);
        }
      }
    }
#undef LOAD_HM
    BARRIER();
#pragma unroll
    for (int ct = 0; ct < 2; ++ct)
#pragma unroll
      for (int k = 0; k < 4; ++k)
        *(LAS f32x4*)(lds + (kq * 64 + ts * 32 + col_token(lane & 31)) * 272 + (32 * ct + 8 * k + 4 * h) * 4) =
            f32x4{acc[ct][4 * k], acc[ct][4 * k + 1], acc[ct][4 * k + 2], acc[ct][4 * k + 3]};
    BARRIER();
    f32x4 r0 = {0.f, 0.f, 0.f, 0.f}, r1 = r0;
#pragma unroll
    for (int k = 0; k < 4; ++k) {
      LAS unsigned char* a = lds + (k * 64 + (tid >> 3)) * 272 + (tid & 7) * 32;
      r0 += *(LAS f32x4*)a; r1 += *(LAS f32x4*)(a + 16);
    }
    const u32 f0 = pack_bf16(r0[0], r0[1]), f1 = pack_bf16(r0[2], r0[3]), f2 = pack_bf16(r1[0], r1[1]), f3 = pack_bf16(r1[2], r1[3]);
    const u32x4 o = {pack_bf16(bf_lo(hr[0]) + bf_lo(f0), bf_hi(hr[0]) + bf_hi(f0)), pack_bf16(bf_lo(hr[1]) + bf_lo(f1), bf_hi(hr[1]) + bf_hi(f1)),
                     pack_bf16(bf_lo(hr[2]) + bf_lo(f2), bf_hi(hr[2]) + bf_hi(f2)), pack_bf16(bf_lo(hr[3]) + bf_lo(f3), bf_hi(hr[3]) + bf_hi(f3))};
    *(GAS u32x4*)(p.out + (size_t)(rb2 * 64 + (tid >> 3)) * DM + db * 64 + (tid & 7) * 8) = o;
    STAMP(8);
  }
}

extern "C" void kernel_launch(void* const* d_in, const int* in_sizes, int n_in, void* d_out, int out_size, void* d_ws, size_t ws_size,
                              hipStream_t stream) {
  Params p;
  p.x = (const u16*)d_in[0]; p.are = (const u16*)d_in[1]; p.aim = (const u16*)d_in[2]; p.bre = (const u16*)d_in[3]; p.bim = (const u16*)d_in[4];
  p.cre = (const u16*)d_in[5]; p.cim = (const u16*)d_in[6]; p.ldt = (const u16*)d_in[7]; p.dsk = (const u16*)d_in[8];
  p.wg = (const u16*)d_in[10]; p.wu = (const u16*)d_in[11]; p.wd = (const u16*)d_in[12];
  p.out = (u16*)d_out; p.ws = (unsigned char*)d_ws;
  fused_kernel<15><<<256, 512, 0, stream>>>(p);
}
```

```cpp
#include <hip/hip_runtime.h>
#include <hip/hip_bf16.h>
#include <stdint.h>

#define STAMPS 0

#define AB_NOWD 0
#define AB_NOREFILL 0
#define AB_NOSTATS 0
#define AB_NOTOK 0
typedef unsigned short u16;
typedef unsigned int u32;
typedef unsigned long long u64;
typedef __attribute__((ext_vector_type(2))) float f32x2;
typedef __attribute__((ext_vector_type(4))) float f32x4;
typedef __attribute__((ext_vector_type(16))) float f32x16;
typedef __attribute__((ext_vector_type(2))) __bf16 bf16x2;
typedef __attribute__((ext_vector_type(8))) __bf16 bf16x8;
typedef __attribute__((ext_vector_type(4))) short s16x4;
typedef __attribute__((ext_vector_type(2))) u32 u32x2;
typedef __attribute__((ext_vector_type(4))) u32 u32x4;
#define LAS __attribute__((address_space(3)))
#define GAS __attribute__((address_space(1)))
#define DI __device__ __forceinline__

constexpr int SEQ = 2048, DM = 448, NST = 256, FF = 1792;
constexpr u32 SENT = 0xFFFFFFFFu;
constexpr int AUX_SC1 = 16;
constexpr u32 SPIN_CAP = 1u << 14;

constexpr int WS_EPOCH = 0;
constexpr int WS_STATS = 4096;
constexpr int WS_FLAG = WS_STATS + SEQ * 8;
constexpr int WS_CS = WS_FLAG + 64 * 128;
constexpr int WS_ST2 = WS_CS + 2 * FF * 8;
constexpr int WS_H2 = 131072;
constexpr int WS_HM = WS_H2 + SEQ * DM * 2;
constexpr int WS_XP = WS_HM + SEQ * FF * 2;
constexpr int WS_DBG = WS_XP + 224 * SEQ * 8;
constexpr int WS_BYTES = WS_DBG + 131072;
static_assert(WS_ST2 + SEQ * 8 <= WS_H2, "control block");

constexpr int LDS_BYTES = 163840;
constexpr int W1_STEP = 16 * 448, W1_SLOTS = 21, W1_EARLY = 10;
constexpr int L_CS = W1_SLOTS * W1_STEP, L_ST = L_CS + 1024;
constexpr int L_SP = L_ST + 1024, L_TAG = LDS_BYTES - 16, L_CNT = LDS_BYTES - 8;
constexpr int L_CV = W1_EARLY * W1_STEP;
constexpr int PQ_ROW = 272, PQ_WAVE = 32 * PQ_ROW;
constexpr int L_U = L_CV + 8 * PQ_WAVE, U_CH = (256 + SEQ) * 2;
constexpr int L_XS = L_U + 2 * U_CH, XS_ROW = 144;
constexpr int L_TAB = L_XS, TAB_CH = 648 * 4;
constexpr int L_XA = L_XS;
constexpr int L_DUMMY = L_XS + 8192;
constexpr int KC_COPY = 260 * 16, KC_CH = 4 * KC_COPY;
constexpr int WD_REG = 7 * W1_STEP;
static_assert(L_DUMMY + 256 <= L_TAG, "conv region");
static_assert(L_ST + 1024 <= LDS_BYTES, "small tables");
#define CODE_LINES_V 256
constexpr int CODE_LINES = CODE_LINES_V;

struct Params {
  const u16 *x, *are, *aim, *bre, *bim, *cre, *cim, *ldt, *dsk, *wg, *wu, *wd;
  u16* out;
  unsigned char* ws;
};

DI u32 pack_bf16(float lo, float hi) { f32x2 v = {lo, hi}; return __builtin_bit_cast(u32, __builtin_convertvector(v, bf16x2)); }
DI u32 pack2(f32x2 v) { return __builtin_bit_cast(u32, __builtin_convertvector(v, bf16x2)); }
DI float bf_lo(u32 x) { return __uint_as_float(x << 16); }
DI float bf_hi(u32 x) { return __uint_as_float(x & 0xffff0000u); }
DI f32x2 unpack2(u32 x) { return f32x2{bf_lo(x), bf_hi(x)}; }
DI float rbf(float x) { return bf_lo(pack_bf16(x, x)); }
DI float dot2(u32 a, u32 b, float c) { return __builtin_amdgcn_fdot2_f32_bf16(__builtin_bit_cast(bf16x2, a), __builtin_bit_cast(bf16x2, b), c, false); }
DI f32x4 mfma16(u32x4 a, u32x4 b, f32x4 c) { return __builtin_amdgcn_mfma_f32_16x16x32_bf16(__builtin_bit_cast(bf16x8, a), __builtin_bit_cast(bf16x8, b), c, 0, 0, 0); }
DI f32x16 mfma32(u32x4 a, u32x4 b, f32x16 c) { return __builtin_amdgcn_mfma_f32_32x32x16_bf16(__builtin_bit_cast(bf16x8, a), __builtin_bit_cast(bf16x8, b), c, 0, 0, 0); }
DI u32x2 lds_tr(LAS unsigned char* p) { return __builtin_bit_cast(u32x2, __builtin_amdgcn_ds_read_tr16_b64_v4i16((LAS s16x4*)p)); }
DI LAS unsigned char* opaque(LAS unsigned char* p) { u32 a = (u32)(size_t)p; asm volatile("" : "+v"(a)); return (LAS unsigned char*)(size_t)a; }
DI void glds16(const void* gsrc, u32 lds_dst) {
  u32 keep;
  asm volatile("s_mov_b32 %0, m0\n\ts_mov_b32 m0, %2\n\ts_nop 0\n\tglobal_load_lds_dwordx4 %1, off\n\ts_mov_b32 m0, %0"
               : "=&s"(keep) : "v"(gsrc), "s"(lds_dst) : "memory");
}
DI void glds4(const void* gsrc, u32 lds_dst) {
  u32 keep;
  asm volatile("s_mov_b32 %0, m0\n\ts_mov_b32 m0, %2\n\ts_nop 0\n\tglobal_load_lds_dword %1, off\n\ts_mov_b32 m0, %0"
               : "=&s"(keep) : "v"(gsrc), "s"(lds_dst) : "memory");
}
#define ALOAD_X4_SC1(dst, voff, srd, soff) asm volatile("buffer_load_dwordx4 %0, %1, %2, %3 offen sc1" : "=v"(dst) : "v"(voff), "s"(srd), "s"(soff) : "memory")
#define ALOAD_X2_SC1(dst, voff, srd) asm volatile("buffer_load_dwordx2 %0, %1, %2, 0 offen sc1" : "=v"(dst) : "v"(voff), "s"(srd) : "memory")
#define VM_WAIT0() do { asm volatile("" ::: "memory"); __builtin_amdgcn_s_waitcnt(0x0F70); asm volatile("s_waitcnt vmcnt(0)" ::: "memory"); } while (0)
#define BARRIER() do { asm volatile("s_waitcnt lgkmcnt(0)" ::: "memory"); __builtin_amdgcn_s_barrier(); asm volatile("" ::: "memory"); } while (0)
#define WAVE_LDS_ORDER() asm volatile("" ::: "memory")

template <int CTRL> DI float dpp_add(float v) {
  return v + __builtin_bit_cast(float, __builtin_amdgcn_update_dpp(0, __builtin_bit_cast(int, v), CTRL, 0xf, 0xf, true));
}
DI float wave_sum(float v) {
  v = dpp_add<0xB1>(v);
  v = dpp_add<0x4E>(v);
  v = dpp_add<0x141>(v);
  v = dpp_add<0x140>(v);
  const int iv = __builtin_bit_cast(int, v);
  const float a = __builtin_bit_cast(float, __builtin_amdgcn_readlane(iv, 0)), b = __builtin_bit_cast(float, __builtin_amdgcn_readlane(iv, 16));
  const float c = __builtin_bit_cast(float, __builtin_amdgcn_readlane(iv, 32)), d = __builtin_bit_cast(float, __builtin_amdgcn_readlane(iv, 48));
  return (a + b) + (c + d);
}
DI float half_sum(float v) {
  const u32 x = __float_as_uint(v);
  auto r = __builtin_amdgcn_permlane32_swap(x, x, false, false);
  return __uint_as_float(r[0]) + __uint_as_float(r[1]);
}
typedef f32x2 cf;
struct cm { cf z, s; };
DI cm mk(cf z) { return {z, cf{-z[1], z[0]}}; }
DI cf cmul(cf a, const cm& b) { return __builtin_elementwise_fma(cf{a[0], a[0]}, b.z, cf{a[1], a[1]} * b.s); }
DI cf zpow(float a, float b, float l) {
  const float m = __builtin_amdgcn_exp2f(a * l);
  const float ph = __builtin_amdgcn_fractf(b * l);
  return cf{m * __builtin_amdgcn_cosf(ph), m * __builtin_amdgcn_sinf(ph)};
}
DI void ln_scalars(float mean, float var, float& m, float& s) {
  m = rbf(mean);
  s = rbf(__builtin_sqrtf(rbf(rbf(var) + 1e-5f)));
}
DI void tok_transpose(u32x4& v) {
  u32 a0 = v[0], a1 = v[1], b0 = v[2], b1 = v[3], t0, t1;
  asm volatile("s_nop 1\n\tv_permlane16_swap_b32 %0, %1\n\tv_permlane16_swap_b32 %2, %3\n\tv_mov_b32 %4, %0\n\tv_mov_b32 %5, %1\n\t"
               "v_mov_b32_dpp %0, %2 row_ror:8 row_mask:0xf bank_mask:0xc\n\tv_mov_b32_dpp %1, %3 row_ror:8 row_mask:0xf bank_mask:0xc\n\t"
               "v_mov_b32_dpp %2, %4 row_ror:8 row_mask:0xf bank_mask:0x3\n\tv_mov_b32_dpp %3, %5 row_ror:8 row_mask:0xf bank_mask:0x3\n\ts_nop 1"
               : "+v"(a0), "+v"(a1), "+v"(b0), "+v"(b1), "=&v"(t0), "=&v"(t1));
  v = u32x4{a0, a1, b0, b1};
}
DI void tok_transpose_a(u32x4& v, u32& t0, u32& t1) {
  u32 a0 = v[0], a1 = v[1], b0 = v[2], b1 = v[3];
  asm volatile("s_nop 1\n\tv_permlane16_swap_b32 %0, %1\n\tv_permlane16_swap_b32 %2, %3\n\tv_mov_b32 %4, %0\n\tv_mov_b32 %5, %1"
               : "+v"(a0), "+v"(a1), "+v"(b0), "+v"(b1), "=&v"(t0), "=&v"(t1));
  v = u32x4{a0, a1, b0, b1};
}
DI void tok_transpose_b(u32x4& v, u32 t0, u32 t1) {
  u32 a0 = v[0], a1 = v[1], b0 = v[2], b1 = v[3];
  asm volatile("s_nop 1\n\tv_mov_b32_dpp %0, %2 row_ror:8 row_mask:0xf bank_mask:0xc\n\tv_mov_b32_dpp %1, %3 row_ror:8 row_mask:0xf bank_mask:0xc\n\t"
               "v_mov_b32_dpp %2, %4 row_ror:8 row_mask:0xf bank_mask:0x3\n\tv_mov_b32_dpp %3, %5 row_ror:8 row_mask:0xf bank_mask:0x3"
               : "+v"(a0), "+v"(a1), "+v"(b0), "+v"(b1) : "v"(t0), "v"(t1));
  v = u32x4{a0, a1, b0, b1};
}
DI int col_token(int j) { return 4 * (j & 7) + 2 * ((j >> 3) & 1) + ((j >> 4) & 1); }
DI int wrap28(int v) { return v >= 28 ? v - 28 : v; }
DI u32 umax3(u32 a, u32 b, u32 c) { return max(max(a, b), c); }

#define STAMP(i) do {} while (0)

constexpr int TOKW = 8, HFW = 12;

template <int NT>
DI void gemm1_wave(const Params& p, LAS unsigned char* lds, u32x4 srd, __amdgpu_buffer_rsrc_t rs, u32 tag, int rb, int cb, int tg, int lane, int rot) {
  const int h = lane >> 5, tid = threadIdx.x;
  u32x2 csg;
  const int cr = tid & 31, cvo = WS_CS + (((cr >> 3) & 1) * FF + cb * 112 + min(tid >> 5, 6) * 16 + (cr >> 4) * 8 + (cr & 7)) * 8;
  ALOAD_X2_SC1(csg, cvo, srd);
  u32x4 tok[28];
  const int tvo = WS_H2 + rb * (224 * 512) + (4 * h + 2 * ((lane >> 3) & 1) + ((lane >> 4) & 1)) * 512 + (tg * 32 + 4 * (lane & 7)) * 4;
#pragma unroll
  for (int s = 0; s < TOKW; ++s) ALOAD_X4_SC1(tok[s], tvo, srd, wrap28(s + rot) * 4096);

  const int cgp = (lane >> 4) & 1, q = (lane & 15) >> 2, pp = lane & 3;
  const int lbase = (8 * h + q) * 448 + (pp >> 1) * 224 + 16 * cgp + 8 * (pp & 1) + (NT == 4 ? 0 : 128);
  LAS unsigned char* abase[3];
#pragma unroll
  for (int k = 0; k < 3; ++k) abase[k] = opaque(lds + lbase + k * WD_REG);
  f32x16 acc[NT];
#pragma unroll
  for (int i = 0; i < NT; ++i)
#pragma unroll
    for (int r = 0; r < 16; ++r) acc[i][r] = 0.f;

  u32x4 wf[2][NT];
#define SB() __builtin_amdgcn_sched_barrier(0)
#define LOAD_WF(s, i0, i1) do { _Pragma("unroll") for (int i = (i0); i < (i1); ++i) { \
      LAS unsigned char* a_ = abase[((s) / 7) % 3] + ((s) % 7) * W1_STEP + i * 32; \
      const u32x2 lo_ = lds_tr(a_), hi_ = lds_tr(a_ + 4 * 448); wf[(s) & 1][i] = u32x4{lo_[0], lo_[1], hi_[0], hi_[1]}; } } while (0)
#define TOK_BAD(s) __ballot(umax3(umax3(tok[s][0], tok[s][1], tok[s][2]), tok[s][3], 0u) == SENT)
#define TOK_WAIT(s, younger) asm volatile("s_waitcnt vmcnt(%1)" : "+v"(tok[s]) : "i"(younger) : "memory")
#define TOK_FIX(s, younger, bad) do { \
    if (__builtin_expect((bad) != 0, 0)) { \
      _Pragma("clang loop unroll(disable)") for (u32 spin = 0; spin < SPIN_CAP; ++spin) { \
        __builtin_amdgcn_s_sleep(2); \
        _Pragma("unroll") for (int t = (s); t <= (s) + (younger); ++t) \
          asm volatile("buffer_load_dwordx4 %0, %1, %2, %3 offen sc1" : "+v"(tok[t]) : "v"(tvo), "s"(srd), "s"(wrap28(t + rot) * 4096) : "memory"); \
        asm volatile("s_waitcnt vmcnt(0)" : "+v"(tok[s]) :: "memory"); \
        if (TOK_BAD(s) == 0) break; \
      } } } while (0)
  LOAD_WF(0, 0, NT);
  asm volatile("s_waitcnt vmcnt(%1)" : "+v"(csg) : "i"(TOKW) : "memory");
  if (NT == 4) {
#pragma clang loop unroll(disable)
    for (u32 spin = 0; spin < SPIN_CAP && __any(csg[1] != tag); ++spin) {
      __builtin_amdgcn_s_sleep(4);
      asm volatile("buffer_load_dwordx2 %0, %1, %2, 0 offen sc1\n\ts_waitcnt vmcnt(0)" : "+v"(csg) : "v"(cvo), "s"(srd) : "memory");
    }
    if (tid < 224) *(LAS u32*)(lds + L_CS + tid * 4) = csg[0];
  }
  TOK_WAIT(0, TOKW - 1);
  { const u64 bad = TOK_BAD(0); TOK_FIX(0, TOKW - 1, bad); }
  tok_transpose(tok[0]);
#pragma unroll
  for (int s = 0; s < 28; ++s) {
    if (s == 1) STAMP(12);
    if (s == 7) STAMP(13);
    if (s == 20) STAMP(14);
    const int young = s + TOKW <= 28 ? TOKW - 2 : 26 - s;
    u64 bad = 0;
    u32 t0 = 0, t1 = 0;
    SB();
    acc[0] = mfma32(wf[s & 1][0], tok[s], acc[0]);
    SB();
    if (s % 7 == 6 && s < 27) BARRIER();
    if (s + 1 < 28) {
      LOAD_WF(s + 1, 0, 1);
      TOK_WAIT(s + 1, young);
      bad = TOK_BAD(s + 1);
    }
    SB();
    acc[1] = mfma32(wf[s & 1][1], tok[s], acc[1]);
    SB();
    if (s + 1 < 28) {
      LOAD_WF(s + 1, 1, 2);
      asm volatile("" : "+s"(bad));
      TOK_FIX(s + 1, young, bad);
      tok_transpose_a(tok[s + 1], t0, t1);
    }
    SB();
    acc[2] = mfma32(wf[s & 1][2], tok[s], acc[2]);
    SB();
    if (s + 1 < 28) { LOAD_WF(s + 1, 2, 3); tok_transpose_b(tok[s + 1], t0, t1); }
    if (NT == 4) {
      SB();
      acc[NT - 1] = mfma32(wf[s & 1][NT - 1], tok[s], acc[NT - 1]);
      SB();
      if (s + 1 < 28) LOAD_WF(s + 1, 3, NT);
    }
    SB();
    if (s + TOKW < 28) { if (AB_NOTOK) tok[s + TOKW] = tok[s]; else ALOAD_X4_SC1(tok[s + TOKW], tvo, srd, wrap28(s + TOKW + rot) * 4096); }
    else asm volatile("s_nop 1");
  }
#undef TOK_BAD
#undef TOK_WAIT
#undef TOK_FIX
#undef LOAD_WF
  STAMP(15);
  BARRIER();
  const f32x2 st = *(LAS f32x2*)(lds + L_ST + (tg * 32 + (lane & 31)) * 8);
  const f32x2 nm = {st[0], st[0]}, r2 = {st[1], st[1]};
#pragma unroll
  for (int i = 0; i < NT; ++i) {
    const int tile = (NT == 4 ? 0 : 4) + i;
    u32x4 o;
#pragma unroll
    for (int k = 0; k < 2; ++k) {
      const f32x4 cg = *(LAS f32x4*)(lds + L_CS + (tile * 32 + 16 * k + 4 * h) * 4), cu = *(LAS f32x4*)(lds + L_CS + (tile * 32 + 16 * k + 8 + 4 * h) * 4);
#pragma unroll
      for (int e = 0; e < 2; ++e) {
        f32x2 g = __builtin_elementwise_fma(nm, f32x2{cg[2 * e], cg[2 * e + 1]}, f32x2{acc[i][8 * k + 2 * e], acc[i][8 * k + 2 * e + 1]});
        const f32x2 u = __builtin_elementwise_fma(nm, f32x2{cu[2 * e], cu[2 * e + 1]}, f32x2{acc[i][8 * k + 4 + 2 * e], acc[i][8 * k + 4 + 2 * e + 1]});
        g = f32x2{fmaxf(g[0], 0.f), fmaxf(g[1], 0.f)};
        o[2 * k + e] = pack2(g * u * r2);
      }
    }
    __builtin_amdgcn_raw_buffer_store_b128(o, rs, WS_HM + (((rb * 4 + tg) * 112 + cb * 7 + tile) * 64 + lane) * 16, 0, AUX_SC1);
  }
}

#define G1MAP 1
#define LOADER_PRIO 3
#define WARM 1
#define ROT 1
#define LOADER_DELAY 4
DI void loader_wave(const Params& p, LAS unsigned char* lds, __amdgpu_buffer_rsrc_t rs, int L, int lane, bool conv_wg, int xcd, int idx, int rb, int cb,
                    int db, u64 pc64, int rot1, int rot2) {
  const unsigned char* ws0;
  const unsigned char* ws1;
  {
    const int pc = L * 64 + lane, row = pc / 28, c = pc % 28;
    ws0 = (const unsigned char*)(c < 14 ? p.wg : p.wu) + row * (FF * 2) + cb * 224 + (c < 14 ? c : c - 14) * 16;
    const int pc1 = pc + 256, row1 = pc1 / 28, c1 = pc1 % 28;
    ws1 = (const unsigned char*)(c1 < 14 ? p.wg : p.wu) + row1 * (FF * 2) + cb * 224 + (c1 < 14 ? c1 : c1 - 14) * 16;
  }
  const unsigned char* ds = (const unsigned char*)p.wd + (size_t)(L * 448 + (lane >> 3)) * (DM * 2) + db * 128 + (((lane & 7) ^ (((lane >> 4) & 1) << 2)) * 16);
#define FILL_W1(ks0, n, slot0) do { _Pragma("clang loop unroll(disable)") for (int k = 0; k < (n); ++k) { \
    const u32 o_ = wrap28((ks0) + k + rot1) * (16 * FF * 2); \
    glds16(ws0 + o_, ((slot0) + k) * W1_STEP + L * 1024); \
    if (L < 3) glds16(ws1 + o_, ((slot0) + k) * W1_STEP + (L + 4) * 1024); } } while (0)
#define FILL_WD(ks0, n, dst0) do { _Pragma("clang loop unroll(disable)") for (int k = 0; k < (n); ++k) { \
    const u32 o_ = wrap28((ks0) + k + rot2) * (16 * DM * 2); \
    glds16(ds + o_, (dst0) + k * 8192 + L * 2048); \
    glds16(ds + o_ + 8 * DM * 2, (dst0) + k * 8192 + L * 2048 + 1024); } } while (0)
  STAMP(0);
  const int t = (idx * 4 + L) * 64 + lane;
  if (CODE_LINES) glds4((const unsigned char*)(pc64 & ~127ull) + (t % (CODE_LINES ? CODE_LINES : 1)) * 128, L_DUMMY);
  __builtin_amdgcn_s_sleep(LOADER_DELAY);
  FILL_W1(0, W1_EARLY, 0);
  if (WARM) {
    const int gl = (int)blockIdx.x * 256 + L * 64 + lane;
    if (gl < 12544) glds4((const unsigned char*)p.wd + gl * 128, L_DUMMY);
  }
  STAMP(1);
  BARRIER();
  if (conv_wg) { BARRIER(); BARRIER(); }
  BARRIER();
  __builtin_amdgcn_s_setprio(LOADER_PRIO);
  STAMP(2);
  u32x4 wr[32];
  const unsigned char* wmat = (const unsigned char*)((L & 1) ? p.wu : p.wg);
  const int e0 = (L >> 1) * 64 + lane;
  {
    u32 go[7];
#pragma unroll
    for (int i = 0; i < 7; ++i) { const u32 e = e0 + 128 * i; go[i] = (e / 14) * (FF * 2) + (e % 14) * 16; asm volatile("" : "+v"(go[i])); }
    const u32 sb = (160 + 16 * rot1) * (FF * 2) + cb * 224;
#pragma unroll
    for (int j = 0; j < 32; ++j) {
      const u32 o = go[j % 7] + (sb + (j / 7) * (64 * FF * 2));
      wr[j] = *(const GAS u32x4*)(wmat + min(o, o - DM * FF * 2));
    }
  }
  STAMP(3);
  __builtin_amdgcn_sched_barrier(0);
  u32 lo[7];
#pragma unroll
  for (int i = 0; i < 7; ++i) { const u32 e = e0 + 128 * i; lo[i] = (e / 14) * 448 + (e % 14) * 16 + (L & 1) * 224; asm volatile("" : "+v"(lo[i])); }
#define PUT_W1(j, row0) *(LAS u32x4*)(size_t)(lo[(j) % 7] + (((j) / 7) * 64 + (row0)) * 448) = wr[j]
  STAMP(4);
  VM_WAIT0();
  STAMP(5);
  BARRIER();
  STAMP(6);
  const u32 tag = *(LAS u32*)(lds + L_TAG);
#pragma unroll
  for (int j = 0; j < 19; ++j) PUT_W1(j, 160);
  if (e0 + 128 * 19 < 2464) PUT_W1(19, 160);
  STAMP(7);
  {
    u32x4 hv[2];
    const int vo = WS_H2 + (rb * 224 + 56 * L + (lane >> 1)) * 512 + (8 * cb + 4 * (lane & 1)) * 4;
#pragma clang loop unroll(disable)
    for (u32 spin = 0; spin < SPIN_CAP; ++spin) {
      hv[0] = __builtin_amdgcn_raw_buffer_load_b128(rs, vo, 0, AUX_SC1);
      hv[1] = __builtin_amdgcn_raw_buffer_load_b128(rs, lane < 48 ? vo + 32 * 512 : WS_BYTES, 0, AUX_SC1);
      const u32 m = umax3(umax3(umax3(hv[0][0], hv[0][1], hv[0][2]), hv[0][3], hv[1][0]), umax3(hv[1][1], hv[1][2], hv[1][3]), 0u);
      if (!__any(m == SENT)) break;
      __builtin_amdgcn_s_sleep(2);
    }
    f32x4 a, q;
#pragma unroll
    for (int e = 0; e < 4; ++e) {
      const f32x2 u = unpack2(hv[0][e]), v = unpack2(hv[1][e]), sm = u + v, sq = u * u + v * v;
      a[e] = dpp_add<0x122>(dpp_add<0x124>(dpp_add<0x128>(sm[0] + sm[1])));
      q[e] = dpp_add<0x122>(dpp_add<0x124>(dpp_add<0x128>(sq[0] + sq[1])));
    }
    if ((lane & 15) < 2) {
      LAS unsigned char* d = lds + L_SP + ((L * 4 + (lane >> 4)) * 2 + (lane & 1)) * 32;
      *(LAS f32x4*)d = a; *(LAS f32x4*)(d + 16) = q;
    }
  }
  STAMP(8);
  VM_WAIT0();
  BARRIER();
  STAMP(9);
  if (L == 0) {
    float a = 0.f, q = 0.f;
#pragma unroll
    for (int k = 0; k < 16; ++k) {
      LAS unsigned char* d = lds + L_SP + (k * 2 + ((lane >> 2) & 1)) * 32 + (lane & 3) * 4;
      a += *(LAS float*)d; q += *(LAS float*)(d + 16);
    }
    a *= 1.f / DM;
    float m, sd; ln_scalars(a, fmaxf(q * (1.f / DM) - a * a, 0.f), m, sd);
    if (lane < 8) __builtin_amdgcn_raw_buffer_store_b64(u32x2{pack_bf16(m, sd), tag}, rs, WS_ST2 + (rb * 128 + 8 * cb + lane) * 8, 0, AUX_SC1);
  }
  if (e0 + 128 * 19 >= 2464) PUT_W1(19, -176);
#pragma unroll
  for (int j = 20; j < 31; ++j) PUT_W1(j, -176);
  if (L < 2) PUT_W1(31, -176);
#undef PUT_W1
  STAMP(10);
  BARRIER();
  STAMP(11);
  if (conv_wg && !AB_NOWD) FILL_WD(0, 6, WD_REG);
  STAMP(12);
  BARRIER();
  STAMP(13);
  {
    u32x2 g;
#pragma clang loop unroll(disable)
    for (u32 spin = 0; spin < SPIN_CAP; ++spin) {
      g = __builtin_amdgcn_raw_buffer_load_b64(rs, WS_ST2 + (rb * 128 + 32 * L + col_token(lane & 31)) * 8, 0, AUX_SC1);
      if (__all(g[1] == tag)) break;
      __builtin_amdgcn_s_sleep(2);
    }
    const float ri = __builtin_amdgcn_rcpf(bf_hi(g[0]));
    if (lane < 32) *(LAS f32x2*)(lds + L_ST + (32 * L + lane) * 8) = f32x2{-bf_lo(g[0]), ri * ri};
  }
  if (conv_wg && !AB_NOWD) FILL_WD(6, 6, 2 * WD_REG);
  STAMP(14);
  BARRIER();
  STAMP(15);
  if (conv_wg) FILL_WD(12, 6, 0);
  VM_WAIT0();
  BARRIER();
  if (!conv_wg) return;
  BARRIER();
  FILL_WD(18, 6, WD_REG);
  VM_WAIT0();
  BARRIER();
  FILL_WD(24, 4, 2 * WD_REG);
  VM_WAIT0();
  BARRIER();
  BARRIER();
  BARRIER();
#undef FILL_W1
#undef FILL_WD
}

__global__ void __launch_bounds__(768, 3) fused_kernel(Params p) {
  u64 pc64;
  asm volatile("s_getpc_b64 %0" : "=s"(pc64));
  __shared__ __attribute__((aligned(16))) unsigned char lds_raw[LDS_BYTES];
  LAS unsigned char* lds = (LAS unsigned char*)lds_raw;
  const int tid = threadIdx.x, lane = tid & 63, w = __builtin_amdgcn_readfirstlane(tid >> 6);
  const int b = blockIdx.x, xcd = b & 7, idx = b >> 3;
  const __amdgpu_buffer_rsrc_t rs = __builtin_amdgcn_make_buffer_rsrc(p.ws, 0, WS_BYTES, 0x00020000);
  const u32x4 srd = {(u32)(size_t)p.ws, (u32)((size_t)p.ws >> 32) & 0xffffu, (u32)WS_BYTES, 0x00020000u};
  const bool conv_wg = idx < 28;
  const int pair = xcd * 28 + (conv_wg ? idx : 0);
  const int rb = 8 * (xcd >> 2) + (idx >> 2), cb = 4 * (xcd & 3) + (idx & 3);
  const int rb2 = 4 * xcd + idx / 7, db = idx % 7;
  const int rot1 = ROT ? wrap28(7 * (cb & 3) + (7 * (rb & 7)) / 2) : 0, rot2 = ROT ? wrap28(4 * db + 7 * (rb2 & 3)) : 0;
  const int ch = w >> 2;
  if (w >= 8) { loader_wave(p, lds, rs, w - 8, lane, conv_wg, xcd, idx, rb, cb, db, pc64, rot1, rot2); return; }
  STAMP(0);

  const int t8 = tid & 255;
  u32 tix[4], kix[11];
#define INDEX_TABLES() do { \
    const int rq = t8 >> 6, q = t8 & 31, hh = (t8 >> 5) & 1; \
    _Pragma("unroll") for (int e = 0; e < 4; ++e) { \
      const int i0 = 8 * q + 4 * hh + e, i1 = hh == 0 ? 256 + 4 * q + e : 384 + 64 * (e >> 1) + 2 * q + (e & 1); \
      const int i2 = hh == 0 ? 512 + 32 * e + q : ((e == 0 && q == 0) ? 640 : 644); \
      tix[e] = L_TAB + ch * TAB_CH + (rq == 0 ? i0 : rq == 1 ? i1 : i2) * 4; \
      asm volatile("" : "+v"(tix[e])); } \
    _Pragma("unroll") for (int e = 0; e < 11; ++e) { \
      const int l = 2047 - 8 * t8 - e; \
      const int bits = (int)(pack_bf16((float)l, 0.f) & 0xffffu) - 17024; \
      kix[e] = L_TAB + ch * TAB_CH + (l < 0 ? 641 : l < 256 ? l : bits) * 4;       \
      asm volatile("" : "+v"(kix[e])); } } while (0)
  u32 tag, pa[7], pd;
  const int po = ((2 * pair + ch) * NST + (w & 3) * 64 + lane) * 2;
#define LOAD_PARAMS() do { \
    asm volatile("global_load_ushort %0, %6, %7\n\tglobal_load_ushort %1, %6, %8\n\tglobal_load_ushort %2, %6, %9\n\t" \
                 "global_load_ushort %3, %6, %10\n\tglobal_load_ushort %4, %6, %11\n\tglobal_load_ushort %5, %6, %12" \
                 : "=&v"(pa[0]), "=&v"(pa[1]), "=&v"(pa[2]), "=&v"(pa[3]), "=&v"(pa[4]), "=&v"(pa[5]) \
                 : "v"(po), "s"(p.are), "s"(p.aim), "s"(p.bre), "s"(p.bim), "s"(p.cre), "s"(p.cim) : "memory"); \
    asm volatile("global_load_ushort %0, %2, %3\n\tglobal_load_dword %1, %4, %5" : "=&v"(pa[6]), "=&v"(pd) \
                 : "v"((2 * pair + ch) * 2), "s"(p.ldt), "v"(pair * 4), "s"(p.dsk) : "memory"); } while (0)
#define WAIT_PARAMS(N) asm volatile("s_waitcnt vmcnt(" #N ")" : "+v"(tag), "+v"(pa[0]), "+v"(pa[1]), "+v"(pa[2]), "+v"(pa[3]), "+v"(pa[4]), "+v"(pa[5]), \
                                    "+v"(pa[6]), "+v"(pd) :: "memory")
  {
    u32x4 sa, xr;
    const int so = (8 * b + (lane >> 3)) * (DM * 2) + w * 112 + min(lane & 7, 6) * 16, ro = (8 * b + w) * (DM * 2) + min(lane, 55) * 16;
    asm volatile("buffer_load_dword %0, off, %3, 0 sc1\n\tglobal_load_dwordx4 %1, %4, %6\n\tglobal_load_dwordx4 %2, %5, %6"
                 : "=&v"(tag), "=&v"(sa), "=&v"(xr) : "s"(srd), "v"(so), "v"(ro), "s"(p.x) : "memory");
    LOAD_PARAMS();
    __builtin_amdgcn_raw_buffer_store_b128(u32x4{SENT, SENT, SENT, SENT}, rs, tid < 448 ? WS_H2 + b * 7168 + tid * 16 : WS_BYTES, 0, AUX_SC1);
    INDEX_TABLES();
    asm volatile("s_waitcnt vmcnt(9)" : "+v"(tag), "+v"(sa), "+v"(xr) :: "memory");
    tag += 1;
    if (tid == 0) { *(LAS u32*)(lds + L_TAG) = tag; *(LAS u32*)(lds + L_CNT) = 0; }
    STAMP(21);
    LAS unsigned char* xs = lds + L_CV + w * PQ_WAVE;
    *(LAS u32x4*)(xs + (lane >> 3) * XS_ROW + (lane & 7) * 16) = sa;
    WAVE_LDS_ORDER();
#pragma unroll
    for (int k = 0; k < 2; ++k) {
      const int e = min(lane + 64 * k, 111), pr = e >> 2, tp = e & 3;
      const u32 x0 = *(LAS u32*)(xs + (2 * tp) * XS_ROW + pr * 4), x1 = *(LAS u32*)(xs + (2 * tp + 1) * XS_ROW + pr * 4);
      __builtin_amdgcn_raw_buffer_store_b128(u32x4{x0, tag, x1, tag}, rs,
                                             lane + 64 * k < 112 ? WS_XP + (w * 28 + pr) * (SEQ * 8) + (8 * b + 2 * tp) * 8 : WS_BYTES, 0, AUX_SC1);
    }
    f32x2 f[4];
#pragma unroll
    for (int e = 0; e < 4; ++e) f[e] = unpack2(xr[e]);
    const f32x2 sv = (f[0] + f[1]) + (f[2] + f[3]);
    const float mean = wave_sum(lane < 56 ? sv[0] + sv[1] : 0.f) * (1.f / DM);
    f32x2 qv = {0.f, 0.f};
#pragma unroll
    for (int e = 0; e < 4; ++e) { const f32x2 d = f[e] - mean; qv += d * d; }
    const float var = wave_sum(lane < 56 ? qv[0] + qv[1] : 0.f) * (1.f / DM);
    float m, sd; ln_scalars(mean, var, m, sd);
    WAIT_PARAMS(2);
    if (lane == 0) __builtin_amdgcn_raw_buffer_store_b64(u32x2{pack_bf16(m, sd), tag}, rs, WS_STATS + (8 * b + w) * 8, 0, AUX_SC1);
  }
  STAMP(1);

  u32x4 af[16];
  u32x4 g[2], x[2];
  const int gvo = w * 2048 + lane * 16;
#define LOAD_GATHER() do { _Pragma("unroll") for (int k = 0; k < 2; ++k) { \
    g[k] = __builtin_amdgcn_raw_buffer_load_b128(rs, WS_STATS + gvo + k * 1024, 0, AUX_SC1); \
    x[k] = __builtin_amdgcn_raw_buffer_load_b128(rs, WS_XP + gvo + k * 1024, pair * (SEQ * 8), AUX_SC1); } } while (0)
  if (conv_wg) {
    if (tid < 64) *(LAS u32x4*)(lds + L_U + (tid >> 5) * U_CH + (tid & 31) * 16) = u32x4{0, 0, 0, 0};
    const float dt = rbf(__expf(bf_lo(pa[6])));
    const float a = dt * bf_lo(pa[0]) * 1.44269504088896341f, bq = dt * bf_lo(pa[1]) * 0.15915494309189535f;
    const cf c0 = cmul(cf{bf_lo(pa[4]), bf_lo(pa[5])}, mk(cf{dt * bf_lo(pa[2]), dt * bf_lo(pa[3])}));
    LAS unsigned char* pq = lds + L_CV + w * PQ_WAVE;
    LAS unsigned char* pqw = opaque(pq + lane * 4);
    LAS unsigned char* pqr = opaque(pq + (lane & 31) * PQ_ROW + (lane >> 5) * 16);
#define SBK() __builtin_amdgcn_sched_barrier(0)
#define CMULS(n, out, x, m) do { cf t_[n]; \
      _Pragma("unroll") for (int k_ = 0; k_ < (n); ++k_) t_[k_] = cf{(x)[k_][1], (x)[k_][1]} * (m)[k_].s; \
      SBK(); \
      _Pragma("unroll") for (int k_ = 0; k_ < (n); ++k_) (out)[k_] = __builtin_elementwise_fma(cf{(x)[k_][0], (x)[k_][0]}, (m)[k_].z, t_[k_]); \
      SBK(); } while (0)
    const cf z8 = zpow(a, bq, 8.f), z1c = zpow(a, -bq, 1.f), w1c = zpow(a, -bq, 256.f);
    {
      const cm m8 = mk(z8);
      const cf z16 = cmul(z8, m8);
      const cm m16 = mk(z16);
      cf e[4] = {cf{1.f, 0.f}, z8, z16, cmul(z16, m8)};
      const cm m32 = mk(cmul(z16, m16));
      const cm mm[4] = {m32, m32, m32, m32};
#pragma unroll
      for (int k = 0; k < 8; ++k) {
#pragma unroll
        for (int i = 0; i < 4; ++i) *(LAS u32*)(pqw + (4 * k + i) * PQ_ROW) = pack2(e[i]);
        if (k < 7) CMULS(4, e, e, mm);
      }
    }
    WAVE_LDS_ORDER();
    u32x4 qf[8];
#pragma unroll
    for (int s = 0; s < 8; ++s) qf[s] = *(LAS u32x4*)(pqr + s * 32);
    WAVE_LDS_ORDER();
    {
      const cm z1 = mk(z1c), w1 = mk(w1c);
      cf pw[8], o[7];
      pw[0] = cf{c0[0], -c0[1]};
      { const cf x[3] = {pw[0], z1c, w1c}; const cm m[3] = {z1, z1, w1}; CMULS(3, o, x, m); }
      pw[1] = o[0];
      const cm z2 = mk(o[1]), w2 = mk(o[2]);
      { const cf x[6] = {pw[0], pw[1], z2.z, w2.z, w2.z, pw[0]}; const cm m[6] = {z2, z2, z2, w1, w2, w1}; CMULS(6, o, x, m); }
      pw[2] = o[0]; pw[3] = o[1];
      const cm z4 = mk(o[2]), w3 = mk(o[3]), w4 = mk(o[4]);
#define PUT_P(row, v) *(LAS u32*)(pqw + (row) * PQ_ROW) = pack2(v)
      PUT_P(0, pw[0]); PUT_P(1, pw[1]); PUT_P(2, pw[2]); PUT_P(3, pw[3]); PUT_P(8, o[5]);
      { const cf x[7] = {pw[0], pw[1], pw[2], pw[3], pw[0], pw[2], pw[0]}; const cm m[7] = {z4, z4, z4, z4, w4, w1, w2}; CMULS(7, o, x, m); }
      pw[4] = o[0]; pw[6] = o[2];
      const cf c4 = o[4];
      PUT_P(4, o[0]); PUT_P(5, o[1]); PUT_P(6, o[2]); PUT_P(7, o[3]); PUT_P(16, o[4]); PUT_P(9, o[5]); PUT_P(12, o[6]);
      { const cf x[5] = {pw[4], pw[6], pw[4], pw[0], pw[4]}; const cm m[5] = {w1, w1, w2, w3, w3}; CMULS(5, o, x, m); }
      PUT_P(10, o[0]); PUT_P(11, o[1]); PUT_P(13, o[2]); PUT_P(14, o[3]); PUT_P(15, o[4]);
      { const cf x[4] = {c4, c4, c4, c4}; const cm m[4] = {w1, w2, w3, w4}; CMULS(4, o, x, m); }
      PUT_P(17, o[0]); PUT_P(18, o[1]); PUT_P(19, o[2]); PUT_P(20, o[3]);
#undef PUT_P
    }
#undef CMULS
    WAVE_LDS_ORDER();
    STAMP(19);
    f32x16 kd;
#pragma unroll
    for (int r = 0; r < 16; ++r) kd[r] = 0.f;
#pragma unroll
    for (int s = 0; s < 8; ++s) kd = mfma32(*(LAS u32x4*)(pqr + s * 32), qf[s], kd);
    WAVE_LDS_ORDER();
#pragma unroll
    for (int k = 0; k < 3; ++k) *(LAS f32x4*)(pq + (k * 64 + lane) * 16) = f32x4{kd[4 * k], kd[4 * k + 1], kd[4 * k + 2], kd[4 * k + 3]};
    STAMP(20);
    BARRIER();
    STAMP(9);
    if (t8 < 192) {
      f32x4 s = *(LAS f32x4*)(lds + L_CV + (ch * 4) * PQ_WAVE + t8 * 16);
#pragma unroll
      for (int k = 1; k < 4; ++k) s += *(LAS f32x4*)(lds + L_CV + (ch * 4 + k) * PQ_WAVE + t8 * 16);
#pragma unroll
      for (int e = 0; e < 4; ++e) *(LAS float*)(size_t)tix[e] = s[e];
    } else if (t8 == 192) *(LAS float*)(lds + L_TAB + ch * TAB_CH + 641 * 4) = 0.f;
    LOAD_GATHER();
    BARRIER();
    STAMP(10);
    {
      float kv[11];
#pragma unroll
      for (int e = 0; e < 11; ++e) kv[e] = *(LAS float*)(size_t)kix[e];
      u32 ev[5], od[5];
#pragma unroll
      for (int m = 0; m < 5; ++m) { ev[m] = pack_bf16(kv[2 * m], kv[2 * m + 1]); od[m] = pack_bf16(kv[2 * m + 1], kv[2 * m + 2]); }
      LAS unsigned char* kc = lds + L_CV + ch * KC_CH + t8 * 16;
#pragma unroll
      for (int sg = 0; sg < 2; ++sg) {
        *(LAS u32x4*)(kc + (2 * sg) * KC_COPY) = u32x4{ev[sg], ev[sg + 1], ev[sg + 2], ev[sg + 3]};
        *(LAS u32x4*)(kc + (2 * sg + 1) * KC_COPY) = u32x4{od[sg], od[sg + 1], od[sg + 2], od[sg + 3]};
      }
      if (t8 < 8) *(LAS u32x4*)(lds + L_CV + ch * KC_CH + (t8 >> 1) * KC_COPY + (256 + (t8 & 1)) * 16) = u32x4{0, 0, 0, 0};
    }
    BARRIER();
    {
      const int r = lane & 15, gq = lane >> 4;
      LAS unsigned char* k0 = lds + L_CV + ch * KC_CH + (3 - (r & 3)) * KC_COPY + (513 - 16 * (w & 3) - 2 * gq - (r >> 2) - 8 - 448) * 8;
      LAS unsigned char* ka = opaque(k0), *kb = opaque(k0 + 8);
#pragma unroll
      for (int c = 0; c < 2; ++c)
#pragma unroll
        for (int i = 0; i < 8; ++i) {
          const int o = (8 + 448 - 8 * c - 64 * i) * 8;
          const u32x2 lo = *(LAS u32x2*)(ka + o), hi = *(LAS u32x2*)(kb + o);
          af[c * 8 + i] = u32x4{lo[0], lo[1], hi[0], hi[1]};
        }
    }
  } else {
    const int j = (idx - 28) * 8 + xcd, pc = tid % 14, rg = min(tid / 14, 31);
    const u16* src = (j < 16 ? p.wg : p.wu) + (size_t)rg * FF + (j & 15) * 112 + pc * 8;
    u32x4 v[14];
#pragma unroll
    for (int k = 0; k < 14; ++k) v[k] = *(const GAS u32x4*)(src + (size_t)k * 32 * FF);
    f32x2 sm[4] = {{0.f, 0.f}, {0.f, 0.f}, {0.f, 0.f}, {0.f, 0.f}};
#pragma unroll
    for (int k = 0; k < 14; ++k)
#pragma unroll
      for (int e = 0; e < 4; ++e) sm[e] += unpack2(v[k][e]);
    if (tid < 448) {
      *(LAS f32x4*)(lds + L_CV + rg * 448 + pc * 32) = f32x4{sm[0][0], sm[0][1], sm[1][0], sm[1][1]};
      *(LAS f32x4*)(lds + L_CV + rg * 448 + pc * 32 + 16) = f32x4{sm[2][0], sm[2][1], sm[3][0], sm[3][1]};
    }
    BARRIER();
    if (tid < 112) {
      float s = 0.f;
#pragma unroll
      for (int k = 0; k < 32; ++k) s += *(LAS float*)(lds + L_CV + k * 448 + tid * 4);
      __builtin_amdgcn_raw_buffer_store_b64(u32x2{__float_as_uint(s), tag}, rs, WS_CS + (j * 112 + tid) * 8, 0, AUX_SC1);
    }
    LOAD_GATHER();
    VM_WAIT0();
  }
  STAMP(2);
  {
#pragma clang loop unroll(disable)
    for (u32 spin = 0; spin < SPIN_CAP; ++spin) {
      bool ok = true;
#pragma unroll
      for (int k = 0; k < 2; ++k) ok = ok && g[k][1] == tag && g[k][3] == tag && x[k][1] == tag && x[k][3] == tag;
      if (__all(ok)) break;
      __builtin_amdgcn_s_sleep(2);
      LOAD_GATHER();
    }
    STAMP(3);
    if (conv_wg) {
#pragma unroll
      for (int k = 0; k < 2; ++k) {
        u32 u[2];
#pragma unroll
        for (int e = 0; e < 2; ++e) {
          const u32 st = g[k][2 * e];
          const u32 d = pack2(unpack2(x[k][2 * e]) - bf_lo(st));
          u[e] = pack2(unpack2(d) * __builtin_amdgcn_rcpf(bf_hi(st)));
        }
        const int l = 256 * w + 128 * k + 2 * lane;
        *(LAS u32*)(lds + L_U + (256 + l) * 2) = __builtin_amdgcn_perm(u[1], u[0], 0x05040100u);
        *(LAS u32*)(lds + L_U + U_CH + (256 + l) * 2) = __builtin_amdgcn_perm(u[1], u[0], 0x07060302u);
        *(LAS u32x2*)(lds + L_XA + l * 4) = u32x2{x[k][0], x[k][2]};
      }
    }
    VM_WAIT0();
  }
#undef LOAD_GATHER
  STAMP(16);
  BARRIER();
  STAMP(17);
  if (tid == 0 && b == 0) __builtin_amdgcn_raw_buffer_store_b32(tag, rs, 0, WS_EPOCH, AUX_SC1);
  if (conv_wg) {
    f32x4 acc[8];
#pragma unroll
    for (int t = 0; t < 8; ++t) acc[t] = f32x4{0.f, 0.f, 0.f, 0.f};
    LAS unsigned char* ub = lds + L_U + ch * U_CH + (256 - 64 * (w & 3) + 16 * (lane & 15) + 8 - 8 * (lane >> 4)) * 2;
#pragma unroll
    for (int j = 0; j < 8; ++j) {
#pragma unroll
      for (int c = 0; c < 2; ++c) {
        const u32x4 bfr = *(LAS u32x4*)(ub + (256 * j - 32 * c) * 2);
#pragma unroll
        for (int i = 0; i + j < 8; ++i) acc[i + j] = mfma16(af[c * 8 + i], bfr, acc[i + j]);
      }
      if (j >= 1) *(LAS f32x4*)(lds + L_CV + w * 8192 + ((j - 1) * 64 + lane) * 16) = acc[j - 1];
    }
    *(LAS f32x4*)(lds + L_CV + w * 8192 + (7 * 64 + lane) * 16) = acc[7];
    const int l0 = 256 * w + 16 * (lane & 15) + 4 * (lane >> 4);
    const u32x4 xc = *(LAS u32x4*)(lds + L_XA + l0 * 4);
    const u32x2 ua = *(LAS u32x2*)(lds + L_U + (256 + l0) * 2), ub2 = *(LAS u32x2*)(lds + L_U + U_CH + (256 + l0) * 2);
    STAMP(18);
    asm volatile("s_waitcnt lgkmcnt(0)" ::: "memory");
    if (lane == 0) __hip_atomic_fetch_add((LAS u32*)(lds + L_CNT), 1u, __ATOMIC_RELAXED, __HIP_MEMORY_SCOPE_WORKGROUP);
#pragma clang loop unroll(disable)
    for (u32 spin = 0; spin < (1u << 20); ++spin)
      if (__builtin_amdgcn_readfirstlane(*(volatile LAS u32*)(lds + L_CNT)) >= 8) break;
    asm volatile("" ::: "memory");
    STAMP(11);
    f32x4 y[2];
#pragma unroll
    for (int c = 0; c < 2; ++c) {
      y[c] = *(LAS f32x4*)(lds + L_CV + (4 * c) * 8192 + tid * 16);
#pragma unroll
      for (int k = 1; k < 4; ++k) y[c] += *(LAS f32x4*)(lds + L_CV + (4 * c + k) * 8192 + tid * 16);
    }
    u32x4 o;
    const f32x2 dsk = unpack2(pd);
#pragma unroll
    for (int e = 0; e < 4; ++e) {
      const f32x2 u = (e & 1) ? f32x2{bf_hi(ua[e >> 1]), bf_hi(ub2[e >> 1])} : f32x2{bf_lo(ua[e >> 1]), bf_lo(ub2[e >> 1])};
      const u32 sk = pack2(u * dsk), yb = pack_bf16(y[0][e], y[1][e]);
      const u32 ys = pack2(unpack2(yb) + unpack2(sk));
      o[e] = min(pack2(unpack2(xc[e]) + unpack2(ys)), SENT - 1);
    }
    __builtin_amdgcn_raw_buffer_store_b128(o, rs, WS_H2 + ((l0 >> 7) * 224 + pair) * 512 + (l0 & 127) * 4, 0, AUX_SC1);
  }
  STAMP(4);
  BARRIER();
  STAMP(5);

  if (w < 4) gemm1_wave<4>(p, lds, srd, rs, tag, rb, cb, w, lane, rot1);
  else gemm1_wave<3>(p, lds, srd, rs, tag, rb, cb, w - 4, lane, rot1);
  STAMP(26);
  VM_WAIT0();
  STAMP(27);
  BARRIER();
  if (tid == 0) __builtin_amdgcn_raw_buffer_store_b32(tag, rs, WS_FLAG + (rb * 4 + (cb >> 2)) * 128 + (cb & 3) * 4, 0, AUX_SC1);
  STAMP(6);

  if (conv_wg) {
    const int kq = w & 3, ts = w >> 2, h = lane >> 5;
    const int cgp = (lane >> 4) & 1, q = (lane & 15) >> 2, pp = lane & 3;
    LAS unsigned char* ab[2][3];
#pragma unroll
    for (int ct = 0; ct < 2; ++ct)
#pragma unroll
      for (int r = 0; r < 3; ++r)
        ab[ct][r] = opaque(lds + ((r + 1) % 3) * WD_REG + kq * 2048 + (4 * h + q) * 128 + ((ct ^ (q >> 1)) * 64) + 32 * cgp + 8 * pp);
#define WD_FRAG(s, ct) ({ LAS unsigned char* a_ = ab[ct][((s) % 18) / 6] + (((s) % 18) % 6) * 8192; const u32x2 lo_ = lds_tr(a_), hi_ = lds_tr(a_ + 1024); \
                          u32x4{lo_[0], lo_[1], hi_[0], hi_[1]}; })
    u32x4 pre[6][2];
#pragma unroll
    for (int s = 0; s < 6; ++s) { pre[s][0] = WD_FRAG(s, 0); pre[s][1] = WD_FRAG(s, 1); }
    BARRIER();
    u32 hr[4];
    const int tk = rb2 * 64 + (tid >> 8) * 32 + col_token((tid >> 3) & 31);
    {
      const int o = WS_H2 + ((tk >> 7) * 224 + db * 32 + (tid & 7) * 4) * 512 + (tk & 127) * 4;
#pragma unroll
      for (int e = 0; e < 4; ++e) hr[e] = __builtin_amdgcn_raw_buffer_load_b32(rs, o + e * 512, 0, AUX_SC1);
    }
#pragma clang loop unroll(disable)
    for (u32 spin = 0; spin < SPIN_CAP; ++spin) {
      const u32x4 f = __builtin_amdgcn_raw_buffer_load_b128(rs, WS_FLAG + ((rb2 >> 1) * 4 + kq) * 128, 0, AUX_SC1);
      if (__all(f[0] == tag && f[1] == tag && f[2] == tag && f[3] == tag)) break;
      __builtin_amdgcn_s_sleep(1);
    }
    __builtin_amdgcn_fence(__ATOMIC_ACQUIRE, "wavefront");
    STAMP(7);
    u32x4 hf[28];
    const int hvo = WS_HM + (((rb2 * 2 + ts) * 112 + kq * 28) * 64 + lane) * 16;
#pragma unroll
    for (int s = 0; s < HFW; ++s) hf[s] = __builtin_amdgcn_raw_buffer_load_b128(rs, hvo, wrap28(s + rot2) * 1024, AUX_SC1);
    __builtin_amdgcn_sched_barrier(0);
    f32x16 acc[2];
#pragma unroll
    for (int ct = 0; ct < 2; ++ct)
#pragma unroll
      for (int r = 0; r < 16; ++r) acc[ct][r] = 0.f;
    u32x4 wn[2][2];
#pragma unroll
    for (int s = 0; s < 28; ++s) {
      if (s == 1) STAMP(22);
      if (s == 10) STAMP(23);
      if (s == 22) STAMP(24);
      if (s == 10 || s == 22) BARRIER();
      __builtin_amdgcn_sched_barrier(0);
      if (s + 1 >= 6 && s + 1 < 28) { wn[(s + 1) & 1][0] = WD_FRAG(s + 1, 0); wn[(s + 1) & 1][1] = WD_FRAG(s + 1, 1); }
#pragma unroll
      for (int ct = 0; ct < 2; ++ct) acc[ct] = mfma32(s < 6 ? pre[s][ct] : wn[s & 1][ct], hf[s], acc[ct]);
      if (s + HFW < 28) hf[s + HFW] = __builtin_amdgcn_raw_buffer_load_b128(rs, hvo, wrap28(s + HFW + rot2) * 1024, AUX_SC1);
      __builtin_amdgcn_sched_barrier(0);
    }
#undef WD_FRAG
    STAMP(25);
    BARRIER();
#pragma unroll
    for (int ct = 0; ct < 2; ++ct)
#pragma unroll
      for (int k = 0; k < 4; ++k)
        *(LAS f32x4*)(lds + (kq * 64 + ts * 32 + (lane & 31)) * 272 + (32 * ct + 8 * k + 4 * h) * 4) =
            f32x4{acc[ct][4 * k], acc[ct][4 * k + 1], acc[ct][4 * k + 2], acc[ct][4 * k + 3]};
    BARRIER();
    f32x4 r0 = {0.f, 0.f, 0.f, 0.f}, r1 = r0;
#pragma unroll
    for (int k = 0; k < 4; ++k) {
      LAS unsigned char* a = lds + (k * 64 + (tid >> 3)) * 272 + (tid & 7) * 32;
      r0 += *(LAS f32x4*)a; r1 += *(LAS f32x4*)(a + 16);
    }
    const u32x4 o = {pack2(unpack2(hr[0]) + unpack2(pack_bf16(r0[0], r0[1]))), pack2(unpack2(hr[1]) + unpack2(pack_bf16(r0[2], r0[3]))),
                     pack2(unpack2(hr[2]) + unpack2(pack_bf16(r1[0], r1[1]))), pack2(unpack2(hr[3]) + unpack2(pack_bf16(r1[2], r1[3])))};
    *(GAS u32x4*)(p.out + (size_t)tk * DM + db * 64 + (tid & 7) * 8) = o;
    STAMP(8);
  }
}

extern "C" void kernel_launch(void* const* d_in, const int* in_sizes, int n_in, void* d_out, int out_size, void* d_ws, size_t ws_size,
                              hipStream_t stream) {
  Params p;
  p.x = (const u16*)d_in[0]; p.are = (const u16*)d_in[1]; p.aim = (const u16*)d_in[2]; p.bre = (const u16*)d_in[3]; p.bim = (const u16*)d_in[4];
  p.cre = (const u16*)d_in[5]; p.cim = (const u16*)d_in[6]; p.ldt = (const u16*)d_in[7]; p.dsk = (const u16*)d_in[8];
  p.wg = (const u16*)d_in[10]; p.wu = (const u16*)d_in[11]; p.wd = (const u16*)d_in[12];
  p.out = (u16*)d_out; p.ws = (unsigned char*)d_ws;
  fused_kernel<<<256, 768, 0, stream>>>(p);
}
```

```cpp
#include <hip/hip_runtime.h>
#include <hip/hip_bf16.h>
#include <stdint.h>

typedef unsigned short u16;
typedef unsigned int u32;
typedef unsigned long long u64;
typedef __attribute__((ext_vector_type(2))) float f32x2;
typedef __attribute__((ext_vector_type(4))) float f32x4;
typedef __attribute__((ext_vector_type(16))) float f32x16;
typedef __attribute__((ext_vector_type(2))) __bf16 bf16x2;
typedef __attribute__((ext_vector_type(8))) __bf16 bf16x8;
typedef __attribute__((ext_vector_type(4))) short s16x4;
typedef __attribute__((ext_vector_type(2))) u32 u32x2;
typedef __attribute__((ext_vector_type(4))) u32 u32x4;
#define LAS __attribute__((address_space(3)))
#define GAS __attribute__((address_space(1)))
#define DI __device__ __forceinline__

constexpr int SEQ = 2048, DM = 448, NST = 256, FF = 1792;
constexpr u32 SENT = 0xFFFFFFFFu;
constexpr int AUX_SC1 = 16;
constexpr u32 SPIN_CAP = 1u << 14;

constexpr int WS_EPOCH = 0;
constexpr int WS_STATS = 4096;
constexpr int WS_FLAG = WS_STATS + SEQ * 8;
constexpr int WS_CS = WS_FLAG + 64 * 128;
constexpr int WS_ST2 = WS_CS + 2 * FF * 8;
constexpr int WS_RF = WS_ST2 + SEQ * 8;
constexpr int WS_H2 = 131072;
constexpr int WS_HM = WS_H2 + SEQ * DM * 2;
constexpr int WS_XP = WS_HM + SEQ * FF * 2;
constexpr int WS_BYTES = WS_XP + 224 * SEQ * 8;
static_assert(WS_RF + 4096 <= WS_H2, "control block");

constexpr int LDS_BYTES = 163840;
constexpr int W1_STEP = 16 * 448, W1_SLOTS = 21, W1_EARLY = 10;
constexpr int L_CS = W1_SLOTS * W1_STEP, L_ST = L_CS + 1024;
constexpr int L_SP = L_ST + 1024, L_CNT = LDS_BYTES - 32;
constexpr int L_CV = W1_EARLY * W1_STEP;
constexpr int PQ_ROW = 272, PQ_WAVE = 32 * PQ_ROW;
constexpr int L_U = L_CV + 8 * PQ_WAVE, U_CH = (256 + SEQ) * 2;
constexpr int L_XA = L_U + 2 * U_CH, XS_ROW = 144;
constexpr int L_DUMMY = L_XA + 8192;
constexpr int KC_OFF = 3072, KC_COPY = PQ_WAVE + 64, KC_CH = 4 * PQ_WAVE;
constexpr int WD_REG = 7 * W1_STEP;
static_assert(L_DUMMY + 256 <= L_CNT, "conv region");
static_assert(L_ST + 1024 <= LDS_BYTES, "small tables");
constexpr int CODE_LINES = 232;

struct KixTab { u16 v[2064]; };
constexpr KixTab make_kix() {
  KixTab t{};
  for (int i = 0; i < 2064; ++i) {
    const int l = 2047 - i;
    int ix = 11 * 64 + 32;
    if (l >= 0) {
      const int sh = l < 256 ? 0 : l < 512 ? 1 : l < 1024 ? 2 : 3;
      const int r = sh ? ((l + (1 << (sh - 1)) - 1 + ((l >> sh) & 1)) >> sh) << sh : l;
      ix = (r < 256 ? ((r >> 2) & 1) * 32 + (r & 3) * 64 : r < 512 ? (4 + ((r >> 1) & 3)) * 64
            : r < 1024 ? (4 + ((r >> 8) & 1) * 2 + ((r >> 2) & 1)) * 64 + 32 : (8 + ((r >> 8) & 3)) * 64 + (r >> 11) * 32) + ((r >> 3) & 31);
    }
    t.v[i] = (u16)(ix * 4);
  }
  return t;
}
__device__ __attribute__((aligned(16))) const KixTab kix_tab = make_kix();

struct Params {
  const u16 *x, *are, *aim, *bre, *bim, *cre, *cim, *ldt, *dsk, *wg, *wu, *wd;
  u16* out;
  unsigned char* ws;
};

DI u32 pack_bf16(float lo, float hi) { f32x2 v = {lo, hi}; return __builtin_bit_cast(u32, __builtin_convertvector(v, bf16x2)); }
DI u32 pack2(f32x2 v) { return __builtin_bit_cast(u32, __builtin_convertvector(v, bf16x2)); }
DI float bf_lo(u32 x) { return __uint_as_float(x << 16); }
DI float bf_hi(u32 x) { return __uint_as_float(x & 0xffff0000u); }
DI f32x2 unpack2(u32 x) { return f32x2{bf_lo(x), bf_hi(x)}; }
DI float rbf(float x) { return bf_lo(pack_bf16(x, x)); }
DI float dot2(u32 a, u32 b, float c) { return __builtin_amdgcn_fdot2_f32_bf16(__builtin_bit_cast(bf16x2, a), __builtin_bit_cast(bf16x2, b), c, false); }
DI f32x4 mfma16(u32x4 a, u32x4 b, f32x4 c) { return __builtin_amdgcn_mfma_f32_16x16x32_bf16(__builtin_bit_cast(bf16x8, a), __builtin_bit_cast(bf16x8, b), c, 0, 0, 0); }
DI f32x16 mfma32(u32x4 a, u32x4 b, f32x16 c) { return __builtin_amdgcn_mfma_f32_32x32x16_bf16(__builtin_bit_cast(bf16x8, a), __builtin_bit_cast(bf16x8, b), c, 0, 0, 0); }
DI u32x2 lds_tr(LAS unsigned char* p) { return __builtin_bit_cast(u32x2, __builtin_amdgcn_ds_read_tr16_b64_v4i16((LAS s16x4*)p)); }
DI LAS unsigned char* opaque(LAS unsigned char* p) { u32 a = (u32)(size_t)p; asm volatile("" : "+v"(a)); return (LAS unsigned char*)(size_t)a; }
DI void glds16(const void* gsrc, u32 lds_dst) {
  u32 keep;
  asm volatile("s_mov_b32 %0, m0\n\ts_mov_b32 m0, %2\n\ts_nop 0\n\tglobal_load_lds_dwordx4 %1, off\n\ts_mov_b32 m0, %0"
               : "=&s"(keep) : "v"(gsrc), "s"(lds_dst) : "memory");
}
DI void glds16s(const void* sbase, u32 voff, u32 lds_dst) {
  u32 keep;
  asm volatile("s_mov_b32 %0, m0\n\ts_mov_b32 m0, %3\n\ts_nop 0\n\tglobal_load_lds_dwordx4 %1, %2\n\ts_mov_b32 m0, %0"
               : "=&s"(keep) : "v"(voff), "s"(sbase), "s"(lds_dst) : "memory");
}
DI void glds4(const void* gsrc, u32 lds_dst) {
  u32 keep;
  asm volatile("s_mov_b32 %0, m0\n\ts_mov_b32 m0, %2\n\ts_nop 0\n\tglobal_load_lds_dword %1, off\n\ts_mov_b32 m0, %0"
               : "=&s"(keep) : "v"(gsrc), "s"(lds_dst) : "memory");
}
#define VM_WAIT0() do { asm volatile("" ::: "memory"); __builtin_amdgcn_s_waitcnt(0x0F70); asm volatile("s_waitcnt vmcnt(0)" ::: "memory"); } while (0)
#define BARRIER() do { asm volatile("s_waitcnt lgkmcnt(0)" ::: "memory"); __builtin_amdgcn_s_barrier(); asm volatile("" ::: "memory"); } while (0)
#define SB() __builtin_amdgcn_sched_barrier(0)
#define WAVE_LDS_ORDER() asm volatile("" ::: "memory")

DI void lds_arrive(u32 cnt) {
  asm volatile("s_waitcnt lgkmcnt(0)\n\ts_mov_b64 exec, 1\n\tds_add_u32 %0, %1\n\ts_mov_b64 exec, -1" :: "v"(cnt), "v"(1u) : "memory");
}
DI void lds_wait(u32 cnt, u32 target) {
  u32 v, sv, spin;
  asm volatile("s_mov_b32 %2, 0x400000\n"
               "W%=:\n\tds_read_b32 %0, %3\n\ts_sub_u32 %2, %2, 1\n\ts_waitcnt lgkmcnt(0)\n\tv_readfirstlane_b32 %1, %0\n\ts_cmp_ge_u32 %1, %4\n\ts_cbranch_scc1 D%=\n\t"
               "s_sleep 1\n\ts_cmp_lg_u32 %2, 0\n\ts_cbranch_scc1 W%=\n"
               "D%=:" : "=&v"(v), "=&s"(sv), "=&s"(spin) : "v"(cnt), "s"(target) : "memory", "scc");
}
#define CSYNC(word, target) do { const u32 c_ = L_CNT + 4 * (word); lds_arrive(c_); lds_wait(c_, target); } while (0)

template <int CTRL> DI float dpp_add(float v) {
  return v + __builtin_bit_cast(float, __builtin_amdgcn_update_dpp(0, __builtin_bit_cast(int, v), CTRL, 0xf, 0xf, true));
}
template <int CTRL, int ROWS> DI float dpp_add_rows(float v) {
  return v + __builtin_bit_cast(float, __builtin_amdgcn_update_dpp(0, __builtin_bit_cast(int, v), CTRL, ROWS, 0xf, false));
}
DI void wave_sum2(float& a, float& b) {
  a = dpp_add<0xB1>(a); b = dpp_add<0xB1>(b);
  a = dpp_add<0x4E>(a); b = dpp_add<0x4E>(b);
  a = dpp_add<0x141>(a); b = dpp_add<0x141>(b);
  a = dpp_add<0x140>(a); b = dpp_add<0x140>(b);
  a = dpp_add_rows<0x142, 0xa>(a); b = dpp_add_rows<0x142, 0xa>(b);
  a = dpp_add_rows<0x143, 0xc>(a); b = dpp_add_rows<0x143, 0xc>(b);
}
DI float half_sum(float v) {
  const u32 x = __float_as_uint(v);
  auto r = __builtin_amdgcn_permlane32_swap(x, x, false, false);
  return __uint_as_float(r[0]) + __uint_as_float(r[1]);
}
typedef f32x2 cf;
struct cm { cf z, s; };
DI cm mk(cf z) { return {z, cf{-z[1], z[0]}}; }
DI cf cmul(cf a, const cm& b) { return __builtin_elementwise_fma(cf{a[0], a[0]}, b.z, cf{a[1], a[1]} * b.s); }
DI cf zpow(float a, float b, float l) {
  const float m = __builtin_amdgcn_exp2f(a * l);
  const float ph = __builtin_amdgcn_fractf(b * l);
  return cf{m * __builtin_amdgcn_cosf(ph), m * __builtin_amdgcn_sinf(ph)};
}
DI void ln_scalars(float mean, float var, float& m, float& s) {
  m = rbf(mean);
  s = rbf(__builtin_amdgcn_sqrtf(rbf(rbf(var) + 1e-5f)));
}
DI int col_token(int j) { return 4 * (j & 7) + 2 * ((j >> 3) & 1) + ((j >> 4) & 1); }
DI int fresh_lane() { int l; asm volatile("v_mbcnt_lo_u32_b32 %0, -1, 0\n\tv_mbcnt_hi_u32_b32 %0, -1, %0" : "=v"(l)); return l; }
DI int wrap28(int v) { return v >= 28 ? v - 28 : v; }
DI u32 umax3(u32 a, u32 b, u32 c) { return max(max(a, b), c); }

constexpr int HFW = 12;

#define G1_LOOP_ASM \
      "s_mov_b32 s64, %[rot]\n\t" \
      "buffer_load_dwordx4 v[116:119], %[tvo], %[srd], s64 offen sc1\n\t" \
      "s_add_u32 s65, s64, 0x1000\n\t" \
      "s_cmp_eq_u32 s65, 0x1c000\n\t" \
      "s_cselect_b32 s65, 0, s65\n\t" \
      "buffer_load_dwordx4 v[120:123], %[tvo], %[srd], s65 offen sc1\n\t" \
      "s_add_u32 s66, s65, 0x1000\n\t" \
      "s_cmp_eq_u32 s66, 0x1c000\n\t" \
      "s_cselect_b32 s66, 0, s66\n\t" \
      "buffer_load_dwordx4 v[124:127], %[tvo], %[srd], s66 offen sc1\n\t" \
      "s_add_u32 s67, s66, 0x1000\n\t" \
      "s_cmp_eq_u32 s67, 0x1c000\n\t" \
      "s_cselect_b32 s67, 0, s67\n\t" \
      "buffer_load_dwordx4 v[128:131], %[tvo], %[srd], s67 offen sc1\n\t" \
      "s_add_u32 s68, s67, 0x1000\n\t" \
      "s_cmp_eq_u32 s68, 0x1c000\n\t" \
      "s_cselect_b32 s68, 0, s68\n\t" \
      "buffer_load_dwordx4 v[132:135], %[tvo], %[srd], s68 offen sc1\n\t" \
      "s_add_u32 s69, s68, 0x1000\n\t" \
      "s_cmp_eq_u32 s69, 0x1c000\n\t" \
      "s_cselect_b32 s69, 0, s69\n\t" \
      "s_add_u32 s70, s69, 0x1000\n\t" \
      "s_cmp_eq_u32 s70, 0x1c000\n\t" \
      "s_cselect_b32 s70, 0, s70\n\t" \
      "s_add_u32 s71, s70, 0x1000\n\t" \
      "s_cmp_eq_u32 s71, 0x1c000\n\t" \
      "s_cselect_b32 s71, 0, s71\n\t" \
      "s_add_u32 s72, s71, 0x1000\n\t" \
      "s_cmp_eq_u32 s72, 0x1c000\n\t" \
      "s_cselect_b32 s72, 0, s72\n\t" \
      "s_add_u32 s73, s72, 0x1000\n\t" \
      "s_cmp_eq_u32 s73, 0x1c000\n\t" \
      "s_cselect_b32 s73, 0, s73\n\t" \
      "s_add_u32 s74, s73, 0x1000\n\t" \
      "s_cmp_eq_u32 s74, 0x1c000\n\t" \
      "s_cselect_b32 s74, 0, s74\n\t" \
      "s_add_u32 s75, s74, 0x1000\n\t" \
      "s_cmp_eq_u32 s75, 0x1c000\n\t" \
      "s_cselect_b32 s75, 0, s75\n\t" \
      "s_add_u32 s76, s75, 0x1000\n\t" \
      "s_cmp_eq_u32 s76, 0x1c000\n\t" \
      "s_cselect_b32 s76, 0, s76\n\t" \
      "s_add_u32 s77, s76, 0x1000\n\t" \
      "s_cmp_eq_u32 s77, 0x1c000\n\t" \
      "s_cselect_b32 s77, 0, s77\n\t" \
      "s_add_u32 s78, s77, 0x1000\n\t" \
      "s_cmp_eq_u32 s78, 0x1c000\n\t" \
      "s_cselect_b32 s78, 0, s78\n\t" \
      "s_add_u32 s79, s78, 0x1000\n\t" \
      "s_cmp_eq_u32 s79, 0x1c000\n\t" \
      "s_cselect_b32 s79, 0, s79\n\t" \
      "s_add_u32 s80, s79, 0x1000\n\t" \
      "s_cmp_eq_u32 s80, 0x1c000\n\t" \
      "s_cselect_b32 s80, 0, s80\n\t" \
      "s_add_u32 s81, s80, 0x1000\n\t" \
      "s_cmp_eq_u32 s81, 0x1c000\n\t" \
      "s_cselect_b32 s81, 0, s81\n\t" \
      "s_add_u32 s82, s81, 0x1000\n\t" \
      "s_cmp_eq_u32 s82, 0x1c000\n\t" \
      "s_cselect_b32 s82, 0, s82\n\t" \
      "s_add_u32 s83, s82, 0x1000\n\t" \
      "s_cmp_eq_u32 s83, 0x1c000\n\t" \
      "s_cselect_b32 s83, 0, s83\n\t" \
      "s_add_u32 s84, s83, 0x1000\n\t" \
      "s_cmp_eq_u32 s84, 0x1c000\n\t" \
      "s_cselect_b32 s84, 0, s84\n\t" \
      "s_add_u32 s85, s84, 0x1000\n\t" \
      "s_cmp_eq_u32 s85, 0x1c000\n\t" \
      "s_cselect_b32 s85, 0, s85\n\t" \
      "s_add_u32 s86, s85, 0x1000\n\t" \
      "s_cmp_eq_u32 s86, 0x1c000\n\t" \
      "s_cselect_b32 s86, 0, s86\n\t" \
      "s_add_u32 s87, s86, 0x1000\n\t" \
      "s_cmp_eq_u32 s87, 0x1c000\n\t" \
      "s_cselect_b32 s87, 0, s87\n\t" \
      "s_add_u32 s88, s87, 0x1000\n\t" \
      "s_cmp_eq_u32 s88, 0x1c000\n\t" \
      "s_cselect_b32 s88, 0, s88\n\t" \
      "s_add_u32 s89, s88, 0x1000\n\t" \
      "s_cmp_eq_u32 s89, 0x1c000\n\t" \
      "s_cselect_b32 s89, 0, s89\n\t" \
      "s_add_u32 s90, s89, 0x1000\n\t" \
      "s_cmp_eq_u32 s90, 0x1c000\n\t" \
      "s_cselect_b32 s90, 0, s90\n\t" \
      "s_add_u32 s91, s90, 0x1000\n\t" \
      "s_cmp_eq_u32 s91, 0x1c000\n\t" \
      "s_cselect_b32 s91, 0, s91\n\t" \
      "ds_read_b64_tr_b16 v[136:137], %[ab] offset:0\n\t" \
      "ds_read_b64_tr_b16 v[138:139], %[ab] offset:1792\n\t" \
      "ds_read_b64_tr_b16 v[140:141], %[ab] offset:32\n\t" \
      "ds_read_b64_tr_b16 v[142:143], %[ab] offset:1824\n\t" \
      "ds_read_b64_tr_b16 v[144:145], %[ab] offset:64\n\t" \
      "ds_read_b64_tr_b16 v[146:147], %[ab] offset:1856\n\t" \
      "ds_read_b64_tr_b16 v[148:149], %[ab] offset:96\n\t" \
      "ds_read_b64_tr_b16 v[150:151], %[ab] offset:1888\n\t" \
      "ds_read_b64_tr_b16 v[152:153], %[ab] offset:128\n\t" \
      "ds_read_b64_tr_b16 v[154:155], %[ab] offset:1920\n\t" \
      "ds_read_b64_tr_b16 v[156:157], %[ab] offset:160\n\t" \
      "ds_read_b64_tr_b16 v[158:159], %[ab] offset:1952\n\t" \
      "ds_read_b64_tr_b16 v[160:161], %[ab] offset:192\n\t" \
      "ds_read_b64_tr_b16 v[162:163], %[ab] offset:1984\n\t" \
      "s_waitcnt vmcnt(4)\n\t" \
      "v_max3_u32 v166, v116, v117, v118\n\t" \
      "v_max_u32 v166, v166, v119\n\t" \
      "s_nop 1\n\t" \
      "v_cmp_eq_u32 vcc, -1, v166\n\t" \
      "s_nop 1\n\t" \
      "s_cbranch_vccnz STUB0_%=\n\t" \
      "BACK0_%=:\n\t" \
      "v_permlane16_swap_b32 v116, v117\n\t" \
      "v_permlane16_swap_b32 v118, v119\n\t" \
      "s_nop 1\n\t" \
      "v_mov_b64 v[164:165], v[116:117]\n\t" \
      "s_nop 1\n\t" \
      "v_mov_b32_dpp v116, v118 row_ror:8 row_mask:0xf bank_mask:0xc\n\t" \
      "v_mov_b32_dpp v117, v119 row_ror:8 row_mask:0xf bank_mask:0xc\n\t" \
      "s_nop 1\n\t" \
      "v_mov_b32_dpp v118, v164 row_ror:8 row_mask:0xf bank_mask:0x3\n\t" \
      "v_mov_b32_dpp v119, v165 row_ror:8 row_mask:0xf bank_mask:0x3\n\t" \
      "s_nop 1\n\t" \
      "s_waitcnt lgkmcnt(8)\n\t" \
      "v_mfma_f32_32x32x16_bf16 v[4:19], v[136:139], v[116:119], 0\n\t" \
      "ds_read_b64_tr_b16 v[136:137], %[ab] offset:7168\n\t" \
      "ds_read_b64_tr_b16 v[138:139], %[ab] offset:8960\n\t" \
      "s_waitcnt vmcnt(3)\n\t" \
      "v_max3_u32 v166, v120, v121, v122\n\t" \
      "v_max_u32 v166, v166, v123\n\t" \
      "v_mfma_f32_32x32x16_bf16 v[20:35], v[140:143], v[116:119], 0\n\t" \
      "ds_read_b64_tr_b16 v[140:141], %[ab] offset:7200\n\t" \
      "ds_read_b64_tr_b16 v[142:143], %[ab] offset:8992\n\t" \
      "v_cmp_eq_u32 vcc, -1, v166\n\t" \
      "v_mfma_f32_32x32x16_bf16 v[36:51], v[144:147], v[116:119], 0\n\t" \
      "ds_read_b64_tr_b16 v[144:145], %[ab] offset:7232\n\t" \
      "ds_read_b64_tr_b16 v[146:147], %[ab] offset:9024\n\t" \
      "s_cbranch_vccnz STUB1_%=\n\t" \
      "BACK1_%=:\n\t" \
      "v_permlane16_swap_b32 v120, v121\n\t" \
      "v_permlane16_swap_b32 v122, v123\n\t" \
      "s_waitcnt lgkmcnt(8)\n\t" \
      "v_mfma_f32_32x32x16_bf16 v[52:67], v[148:151], v[116:119], 0\n\t" \
      "ds_read_b64_tr_b16 v[148:149], %[ab] offset:7264\n\t" \
      "ds_read_b64_tr_b16 v[150:151], %[ab] offset:9056\n\t" \
      "v_mov_b64 v[164:165], v[120:121]\n\t" \
      "v_mfma_f32_32x32x16_bf16 v[68:83], v[152:155], v[116:119], 0\n\t" \
      "ds_read_b64_tr_b16 v[152:153], %[ab] offset:7296\n\t" \
      "ds_read_b64_tr_b16 v[154:155], %[ab] offset:9088\n\t" \
      "v_mov_b32_dpp v120, v122 row_ror:8 row_mask:0xf bank_mask:0xc\n\t" \
      "v_mov_b32_dpp v121, v123 row_ror:8 row_mask:0xf bank_mask:0xc\n\t" \
      "v_mfma_f32_32x32x16_bf16 v[84:99], v[156:159], v[116:119], 0\n\t" \
      "ds_read_b64_tr_b16 v[156:157], %[ab] offset:7328\n\t" \
      "ds_read_b64_tr_b16 v[158:159], %[ab] offset:9120\n\t" \
      "v_mov_b32_dpp v122, v164 row_ror:8 row_mask:0xf bank_mask:0x3\n\t" \
      "v_mov_b32_dpp v123, v165 row_ror:8 row_mask:0xf bank_mask:0x3\n\t" \
      "s_waitcnt lgkmcnt(12)\n\t" \
      "v_mfma_f32_32x32x16_bf16 v[100:115], v[160:163], v[116:119], 0\n\t" \
      "ds_read_b64_tr_b16 v[160:161], %[ab] offset:7360\n\t" \
      "ds_read_b64_tr_b16 v[162:163], %[ab] offset:9152\n\t" \
      "buffer_load_dwordx4 v[116:119], %[tvo], %[srd], s69 offen sc1\n\t" \
      "s_waitcnt lgkmcnt(8)\n\t" \
      "v_mfma_f32_32x32x16_bf16 v[4:19], v[136:139], v[120:123], v[4:19]\n\t" \
      "ds_read_b64_tr_b16 v[136:137], %[ab] offset:14336\n\t" \
      "ds_read_b64_tr_b16 v[138:139], %[ab] offset:16128\n\t" \
      "s_waitcnt vmcnt(3)\n\t" \
      "v_max3_u32 v166, v124, v125, v126\n\t" \
      "v_max_u32 v166, v166, v127\n\t" \
      "v_mfma_f32_32x32x16_bf16 v[20:35], v[140:143], v[120:123], v[20:35]\n\t" \
      "ds_read_b64_tr_b16 v[140:141], %[ab] offset:14368\n\t" \
      "ds_read_b64_tr_b16 v[142:143], %[ab] offset:16160\n\t" \
      "v_cmp_eq_u32 vcc, -1, v166\n\t" \
      "v_mfma_f32_32x32x16_bf16 v[36:51], v[144:147], v[120:123], v[36:51]\n\t" \
      "ds_read_b64_tr_b16 v[144:145], %[ab] offset:14400\n\t" \
      "ds_read_b64_tr_b16 v[146:147], %[ab] offset:16192\n\t" \
      "s_cbranch_vccnz STUB2_%=\n\t" \
      "BACK2_%=:\n\t" \
      "v_permlane16_swap_b32 v124, v125\n\t" \
      "v_permlane16_swap_b32 v126, v127\n\t" \
      "s_waitcnt lgkmcnt(8)\n\t" \
      "v_mfma_f32_32x32x16_bf16 v[52:67], v[148:151], v[120:123], v[52:67]\n\t" \
      "ds_read_b64_tr_b16 v[148:149], %[ab] offset:14432\n\t" \
      "ds_read_b64_tr_b16 v[150:151], %[ab] offset:16224\n\t" \
      "v_mov_b64 v[164:165], v[124:125]\n\t" \
      "v_mfma_f32_32x32x16_bf16 v[68:83], v[152:155], v[120:123], v[68:83]\n\t" \
      "ds_read_b64_tr_b16 v[152:153], %[ab] offset:14464\n\t" \
      "ds_read_b64_tr_b16 v[154:155], %[ab] offset:16256\n\t" \
      "v_mov_b32_dpp v124, v126 row_ror:8 row_mask:0xf bank_mask:0xc\n\t" \
      "v_mov_b32_dpp v125, v127 row_ror:8 row_mask:0xf bank_mask:0xc\n\t" \
      "v_mfma_f32_32x32x16_bf16 v[84:99], v[156:159], v[120:123], v[84:99]\n\t" \
      "ds_read_b64_tr_b16 v[156:157], %[ab] offset:14496\n\t" \
      "ds_read_b64_tr_b16 v[158:159], %[ab] offset:16288\n\t" \
      "v_mov_b32_dpp v126, v164 row_ror:8 row_mask:0xf bank_mask:0x3\n\t" \
      "v_mov_b32_dpp v127, v165 row_ror:8 row_mask:0xf bank_mask:0x3\n\t" \
      "s_waitcnt lgkmcnt(12)\n\t" \
      "v_mfma_f32_32x32x16_bf16 v[100:115], v[160:163], v[120:123], v[100:115]\n\t" \
      "ds_read_b64_tr_b16 v[160:161], %[ab] offset:14528\n\t" \
      "ds_read_b64_tr_b16 v[162:163], %[ab] offset:16320\n\t" \
      "buffer_load_dwordx4 v[120:123], %[tvo], %[srd], s70 offen sc1\n\t" \
      "s_waitcnt lgkmcnt(8)\n\t" \
      "v_mfma_f32_32x32x16_bf16 v[4:19], v[136:139], v[124:127], v[4:19]\n\t" \
      "ds_read_b64_tr_b16 v[136:137], %[ab] offset:21504\n\t" \
      "ds_read_b64_tr_b16 v[138:139], %[ab] offset:23296\n\t" \
      "s_waitcnt vmcnt(3)\n\t" \
      "v_max3_u32 v166, v128, v129, v130\n\t" \
      "v_max_u32 v166, v166, v131\n\t" \
      "v_mfma_f32_32x32x16_bf16 v[20:35], v[140:143], v[124:127], v[20:35]\n\t" \
      "ds_read_b64_tr_b16 v[140:141], %[ab] offset:21536\n\t" \
      "ds_read_b64_tr_b16 v[142:143], %[ab] offset:23328\n\t" \
      "v_cmp_eq_u32 vcc, -1, v166\n\t" \
      "v_mfma_f32_32x32x16_bf16 v[36:51], v[144:147], v[124:127], v[36:51]\n\t" \
      "ds_read_b64_tr_b16 v[144:145], %[ab] offset:21568\n\t" \
      "ds_read_b64_tr_b16 v[146:147], %[ab] offset:23360\n\t" \
      "s_cbranch_vccnz STUB3_%=\n\t" \
      "BACK3_%=:\n\t" \
      "v_permlane16_swap_b32 v128, v129\n\t" \
      "v_permlane16_swap_b32 v130, v131\n\t" \
      "s_waitcnt lgkmcnt(8)\n\t" \
      "v_mfma_f32_32x32x16_bf16 v[52:67], v[148:151], v[124:127], v[52:67]\n\t" \
      "ds_read_b64_tr_b16 v[148:149], %[ab] offset:21600\n\t" \
      "ds_read_b64_tr_b16 v[150:151], %[ab] offset:23392\n\t" \
      "v_mov_b64 v[164:165], v[128:129]\n\t" \
      "v_mfma_f32_32x32x16_bf16 v[68:83], v[152:155], v[124:127], v[68:83]\n\t" \
      "ds_read_b64_tr_b16 v[152:153], %[ab] offset:21632\n\t" \
      "ds_read_b64_tr_b16 v[154:155], %[ab] offset:23424\n\t" \
      "v_mov_b32_dpp v128, v130 row_ror:8 row_mask:0xf bank_mask:0xc\n\t" \
      "v_mov_b32_dpp v129, v131 row_ror:8 row_mask:0xf bank_mask:0xc\n\t" \
      "v_mfma_f32_32x32x16_bf16 v[84:99], v[156:159], v[124:127], v[84:99]\n\t" \
      "ds_read_b64_tr_b16 v[156:157], %[ab] offset:21664\n\t" \
      "ds_read_b64_tr_b16 v[158:159], %[ab] offset:23456\n\t" \
      "v_mov_b32_dpp v130, v164 row_ror:8 row_mask:0xf bank_mask:0x3\n\t" \
      "v_mov_b32_dpp v131, v165 row_ror:8 row_mask:0xf bank_mask:0x3\n\t" \
      "s_waitcnt lgkmcnt(12)\n\t" \
      "v_mfma_f32_32x32x16_bf16 v[100:115], v[160:163], v[124:127], v[100:115]\n\t" \
      "ds_read_b64_tr_b16 v[160:161], %[ab] offset:21696\n\t" \
      "ds_read_b64_tr_b16 v[162:163], %[ab] offset:23488\n\t" \
      "buffer_load_dwordx4 v[124:127], %[tvo], %[srd], s71 offen sc1\n\t" \
      "s_waitcnt lgkmcnt(8)\n\t" \
      "v_mfma_f32_32x32x16_bf16 v[4:19], v[136:139], v[128:131], v[4:19]\n\t" \
      "ds_read_b64_tr_b16 v[136:137], %[ab] offset:28672\n\t" \
      "ds_read_b64_tr_b16 v[138:139], %[ab] offset:30464\n\t" \
      "s_waitcnt vmcnt(3)\n\t" \
      "v_max3_u32 v166, v132, v133, v134\n\t" \
      "v_max_u32 v166, v166, v135\n\t" \
      "v_mfma_f32_32x32x16_bf16 v[20:35], v[140:143], v[128:131], v[20:35]\n\t" \
      "ds_read_b64_tr_b16 v[140:141], %[ab] offset:28704\n\t" \
      "ds_read_b64_tr_b16 v[142:143], %[ab] offset:30496\n\t" \
      "v_cmp_eq_u32 vcc, -1, v166\n\t" \
      "v_mfma_f32_32x32x16_bf16 v[36:51], v[144:147], v[128:131], v[36:51]\n\t" \
      "ds_read_b64_tr_b16 v[144:145], %[ab] offset:28736\n\t" \
      "ds_read_b64_tr_b16 v[146:147], %[ab] offset:30528\n\t" \
      "s_cbranch_vccnz STUB4_%=\n\t" \
      "BACK4_%=:\n\t" \
      "v_permlane16_swap_b32 v132, v133\n\t" \
      "v_permlane16_swap_b32 v134, v135\n\t" \
      "s_waitcnt lgkmcnt(8)\n\t" \
      "v_mfma_f32_32x32x16_bf16 v[52:67], v[148:151], v[128:131], v[52:67]\n\t" \
      "ds_read_b64_tr_b16 v[148:149], %[ab] offset:28768\n\t" \
      "ds_read_b64_tr_b16 v[150:151], %[ab] offset:30560\n\t" \
      "v_mov_b64 v[164:165], v[132:133]\n\t" \
      "v_mfma_f32_32x32x16_bf16 v[68:83], v[152:155], v[128:131], v[68:83]\n\t" \
      "ds_read_b64_tr_b16 v[152:153], %[ab] offset:28800\n\t" \
      "ds_read_b64_tr_b16 v[154:155], %[ab] offset:30592\n\t" \
      "v_mov_b32_dpp v132, v134 row_ror:8 row_mask:0xf bank_mask:0xc\n\t" \
      "v_mov_b32_dpp v133, v135 row_ror:8 row_mask:0xf bank_mask:0xc\n\t" \
      "v_mfma_f32_32x32x16_bf16 v[84:99], v[156:159], v[128:131], v[84:99]\n\t" \
      "ds_read_b64_tr_b16 v[156:157], %[ab] offset:28832\n\t" \
      "ds_read_b64_tr_b16 v[158:159], %[ab] offset:30624\n\t" \
      "v_mov_b32_dpp v134, v164 row_ror:8 row_mask:0xf bank_mask:0x3\n\t" \
      "v_mov_b32_dpp v135, v165 row_ror:8 row_mask:0xf bank_mask:0x3\n\t" \
      "s_waitcnt lgkmcnt(12)\n\t" \
      "v_mfma_f32_32x32x16_bf16 v[100:115], v[160:163], v[128:131], v[100:115]\n\t" \
      "ds_read_b64_tr_b16 v[160:161], %[ab] offset:28864\n\t" \
      "ds_read_b64_tr_b16 v[162:163], %[ab] offset:30656\n\t" \
      "buffer_load_dwordx4 v[128:131], %[tvo], %[srd], s72 offen sc1\n\t" \
      "s_waitcnt lgkmcnt(8)\n\t" \
      "v_mfma_f32_32x32x16_bf16 v[4:19], v[136:139], v[132:135], v[4:19]\n\t" \
      "ds_read_b64_tr_b16 v[136:137], %[ab] offset:35840\n\t" \
      "ds_read_b64_tr_b16 v[138:139], %[ab] offset:37632\n\t" \
      "s_waitcnt vmcnt(3)\n\t" \
      "v_max3_u32 v166, v116, v117, v118\n\t" \
      "v_max_u32 v166, v166, v119\n\t" \
      "v_mfma_f32_32x32x16_bf16 v[20:35], v[140:143], v[132:135], v[20:35]\n\t" \
      "ds_read_b64_tr_b16 v[140:141], %[ab] offset:35872\n\t" \
      "ds_read_b64_tr_b16 v[142:143], %[ab] offset:37664\n\t" \
      "v_cmp_eq_u32 vcc, -1, v166\n\t" \
      "v_mfma_f32_32x32x16_bf16 v[36:51], v[144:147], v[132:135], v[36:51]\n\t" \
      "ds_read_b64_tr_b16 v[144:145], %[ab] offset:35904\n\t" \
      "ds_read_b64_tr_b16 v[146:147], %[ab] offset:37696\n\t" \
      "s_cbranch_vccnz STUB5_%=\n\t" \
      "BACK5_%=:\n\t" \
      "v_permlane16_swap_b32 v116, v117\n\t" \
      "v_permlane16_swap_b32 v118, v119\n\t" \
      "s_waitcnt lgkmcnt(8)\n\t" \
      "v_mfma_f32_32x32x16_bf16 v[52:67], v[148:151], v[132:135], v[52:67]\n\t" \
      "ds_read_b64_tr_b16 v[148:149], %[ab] offset:35936\n\t" \
      "ds_read_b64_tr_b16 v[150:151], %[ab] offset:37728\n\t" \
      "v_mov_b64 v[164:165], v[116:117]\n\t" \
      "v_mfma_f32_32x32x16_bf16 v[68:83], v[152:155], v[132:135], v[68:83]\n\t" \
      "ds_read_b64_tr_b16 v[152:153], %[ab] offset:35968\n\t" \
      "ds_read_b64_tr_b16 v[154:155], %[ab] offset:37760\n\t" \
      "v_mov_b32_dpp v116, v118 row_ror:8 row_mask:0xf bank_mask:0xc\n\t" \
      "v_mov_b32_dpp v117, v119 row_ror:8 row_mask:0xf bank_mask:0xc\n\t" \
      "v_mfma_f32_32x32x16_bf16 v[84:99], v[156:159], v[132:135], v[84:99]\n\t" \
      "ds_read_b64_tr_b16 v[156:157], %[ab] offset:36000\n\t" \
      "ds_read_b64_tr_b16 v[158:159], %[ab] offset:37792\n\t" \
      "v_mov_b32_dpp v118, v164 row_ror:8 row_mask:0xf bank_mask:0x3\n\t" \
      "v_mov_b32_dpp v119, v165 row_ror:8 row_mask:0xf bank_mask:0x3\n\t" \
      "s_waitcnt lgkmcnt(12)\n\t" \
      "v_mfma_f32_32x32x16_bf16 v[100:115], v[160:163], v[132:135], v[100:115]\n\t" \
      "ds_read_b64_tr_b16 v[160:161], %[ab] offset:36032\n\t" \
      "ds_read_b64_tr_b16 v[162:163], %[ab] offset:37824\n\t" \
      "buffer_load_dwordx4 v[132:135], %[tvo], %[srd], s73 offen sc1\n\t" \
      "s_waitcnt lgkmcnt(8)\n\t" \
      "v_mfma_f32_32x32x16_bf16 v[4:19], v[136:139], v[116:119], v[4:19]\n\t" \
      "ds_read_b64_tr_b16 v[136:137], %[ab] offset:43008\n\t" \
      "ds_read_b64_tr_b16 v[138:139], %[ab] offset:44800\n\t" \
      "s_waitcnt vmcnt(3)\n\t" \
      "v_max3_u32 v166, v120, v121, v122\n\t" \
      "v_max_u32 v166, v166, v123\n\t" \
      "v_mfma_f32_32x32x16_bf16 v[20:35], v[140:143], v[116:119], v[20:35]\n\t" \
      "ds_read_b64_tr_b16 v[140:141], %[ab] offset:43040\n\t" \
      "ds_read_b64_tr_b16 v[142:143], %[ab] offset:44832\n\t" \
      "v_cmp_eq_u32 vcc, -1, v166\n\t" \
      "v_mfma_f32_32x32x16_bf16 v[36:51], v[144:147], v[116:119], v[36:51]\n\t" \
      "ds_read_b64_tr_b16 v[144:145], %[ab] offset:43072\n\t" \
      "ds_read_b64_tr_b16 v[146:147], %[ab] offset:44864\n\t" \
      "s_cbranch_vccnz STUB6_%=\n\t" \
      "BACK6_%=:\n\t" \
      "v_permlane16_swap_b32 v120, v121\n\t" \
      "v_permlane16_swap_b32 v122, v123\n\t" \
      "s_waitcnt lgkmcnt(8)\n\t" \
      "v_mfma_f32_32x32x16_bf16 v[52:67], v[148:151], v[116:119], v[52:67]\n\t" \
      "ds_read_b64_tr_b16 v[148:149], %[ab] offset:43104\n\t" \
      "ds_read_b64_tr_b16 v[150:151], %[ab] offset:44896\n\t" \
      "v_mov_b64 v[164:165], v[120:121]\n\t" \
      "v_mfma_f32_32x32x16_bf16 v[68:83], v[152:155], v[116:119], v[68:83]\n\t" \
      "ds_read_b64_tr_b16 v[152:153], %[ab] offset:43136\n\t" \
      "ds_read_b64_tr_b16 v[154:155], %[ab] offset:44928\n\t" \
      "v_mov_b32_dpp v120, v122 row_ror:8 row_mask:0xf bank_mask:0xc\n\t" \
      "v_mov_b32_dpp v121, v123 row_ror:8 row_mask:0xf bank_mask:0xc\n\t" \
      "v_mfma_f32_32x32x16_bf16 v[84:99], v[156:159], v[116:119], v[84:99]\n\t" \
      "ds_read_b64_tr_b16 v[156:157], %[ab] offset:43168\n\t" \
      "ds_read_b64_tr_b16 v[158:159], %[ab] offset:44960\n\t" \
      "v_mov_b32_dpp v122, v164 row_ror:8 row_mask:0xf bank_mask:0x3\n\t" \
      "v_mov_b32_dpp v123, v165 row_ror:8 row_mask:0xf bank_mask:0x3\n\t" \
      "s_waitcnt lgkmcnt(12)\n\t" \
      "v_mfma_f32_32x32x16_bf16 v[100:115], v[160:163], v[116:119], v[100:115]\n\t" \
      "ds_read_b64_tr_b16 v[160:161], %[ab] offset:43200\n\t" \
      "ds_read_b64_tr_b16 v[162:163], %[ab] offset:44992\n\t" \
      "buffer_load_dwordx4 v[116:119], %[tvo], %[srd], s74 offen sc1\n\t" \
      "s_waitcnt lgkmcnt(8)\n\t" \
      "v_mfma_f32_32x32x16_bf16 v[4:19], v[136:139], v[120:123], v[4:19]\n\t" \
      "s_waitcnt lgkmcnt(0)\n\t" \
      "s_barrier\n\t" \
      "v_add_u32 %[ab], 50176, %[ab]\n\t" \
      "ds_read_b64_tr_b16 v[136:137], %[ab] offset:0\n\t" \
      "ds_read_b64_tr_b16 v[138:139], %[ab] offset:1792\n\t" \
      "s_waitcnt vmcnt(3)\n\t" \
      "v_max3_u32 v166, v124, v125, v126\n\t" \
      "v_max_u32 v166, v166, v127\n\t" \
      "v_mfma_f32_32x32x16_bf16 v[20:35], v[140:143], v[120:123], v[20:35]\n\t" \
      "ds_read_b64_tr_b16 v[140:141], %[ab] offset:32\n\t" \
      "ds_read_b64_tr_b16 v[142:143], %[ab] offset:1824\n\t" \
      "v_cmp_eq_u32 vcc, -1, v166\n\t" \
      "v_mfma_f32_32x32x16_bf16 v[36:51], v[144:147], v[120:123], v[36:51]\n\t" \
      "ds_read_b64_tr_b16 v[144:145], %[ab] offset:64\n\t" \
      "ds_read_b64_tr_b16 v[146:147], %[ab] offset:1856\n\t" \
      "s_cbranch_vccnz STUB7_%=\n\t" \
      "BACK7_%=:\n\t" \
      "v_permlane16_swap_b32 v124, v125\n\t" \
      "v_permlane16_swap_b32 v126, v127\n\t" \
      "v_mfma_f32_32x32x16_bf16 v[52:67], v[148:151], v[120:123], v[52:67]\n\t" \
      "ds_read_b64_tr_b16 v[148:149], %[ab] offset:96\n\t" \
      "ds_read_b64_tr_b16 v[150:151], %[ab] offset:1888\n\t" \
      "v_mov_b64 v[164:165], v[124:125]\n\t" \
      "v_mfma_f32_32x32x16_bf16 v[68:83], v[152:155], v[120:123], v[68:83]\n\t" \
      "ds_read_b64_tr_b16 v[152:153], %[ab] offset:128\n\t" \
      "ds_read_b64_tr_b16 v[154:155], %[ab] offset:1920\n\t" \
      "v_mov_b32_dpp v124, v126 row_ror:8 row_mask:0xf bank_mask:0xc\n\t" \
      "v_mov_b32_dpp v125, v127 row_ror:8 row_mask:0xf bank_mask:0xc\n\t" \
      "v_mfma_f32_32x32x16_bf16 v[84:99], v[156:159], v[120:123], v[84:99]\n\t" \
      "ds_read_b64_tr_b16 v[156:157], %[ab] offset:160\n\t" \
      "ds_read_b64_tr_b16 v[158:159], %[ab] offset:1952\n\t" \
      "v_mov_b32_dpp v126, v164 row_ror:8 row_mask:0xf bank_mask:0x3\n\t" \
      "v_mov_b32_dpp v127, v165 row_ror:8 row_mask:0xf bank_mask:0x3\n\t" \
      "v_mfma_f32_32x32x16_bf16 v[100:115], v[160:163], v[120:123], v[100:115]\n\t" \
      "ds_read_b64_tr_b16 v[160:161], %[ab] offset:192\n\t" \
      "ds_read_b64_tr_b16 v[162:163], %[ab] offset:1984\n\t" \
      "buffer_load_dwordx4 v[120:123], %[tvo], %[srd], s75 offen sc1\n\t" \
      "s_waitcnt lgkmcnt(8)\n\t" \
      "v_mfma_f32_32x32x16_bf16 v[4:19], v[136:139], v[124:127], v[4:19]\n\t" \
      "ds_read_b64_tr_b16 v[136:137], %[ab] offset:7168\n\t" \
      "ds_read_b64_tr_b16 v[138:139], %[ab] offset:8960\n\t" \
      "s_waitcnt vmcnt(3)\n\t" \
      "v_max3_u32 v166, v128, v129, v130\n\t" \
      "v_max_u32 v166, v166, v131\n\t" \
      "v_mfma_f32_32x32x16_bf16 v[20:35], v[140:143], v[124:127], v[20:35]\n\t" \
      "ds_read_b64_tr_b16 v[140:141], %[ab] offset:7200\n\t" \
      "ds_read_b64_tr_b16 v[142:143], %[ab] offset:8992\n\t" \
      "v_cmp_eq_u32 vcc, -1, v166\n\t" \
      "v_mfma_f32_32x32x16_bf16 v[36:51], v[144:147], v[124:127], v[36:51]\n\t" \
      "ds_read_b64_tr_b16 v[144:145], %[ab] offset:7232\n\t" \
      "ds_read_b64_tr_b16 v[146:147], %[ab] offset:9024\n\t" \
      "s_cbranch_vccnz STUB8_%=\n\t" \
      "BACK8_%=:\n\t" \
      "v_permlane16_swap_b32 v128, v129\n\t" \
      "v_permlane16_swap_b32 v130, v131\n\t" \
      "s_waitcnt lgkmcnt(8)\n\t" \
      "v_mfma_f32_32x32x16_bf16 v[52:67], v[148:151], v[124:127], v[52:67]\n\t" \
      "ds_read_b64_tr_b16 v[148:149], %[ab] offset:7264\n\t" \
      "ds_read_b64_tr_b16 v[150:151], %[ab] offset:9056\n\t" \
      "v_mov_b64 v[164:165], v[128:129]\n\t" \
      "v_mfma_f32_32x32x16_bf16 v[68:83], v[152:155], v[124:127], v[68:83]\n\t" \
      "ds_read_b64_tr_b16 v[152:153], %[ab] offset:7296\n\t" \
      "ds_read_b64_tr_b16 v[154:155], %[ab] offset:9088\n\t" \
      "v_mov_b32_dpp v128, v130 row_ror:8 row_mask:0xf bank_mask:0xc\n\t" \
      "v_mov_b32_dpp v129, v131 row_ror:8 row_mask:0xf bank_mask:0xc\n\t" \
      "v_mfma_f32_32x32x16_bf16 v[84:99], v[156:159], v[124:127], v[84:99]\n\t" \
      "ds_read_b64_tr_b16 v[156:157], %[ab] offset:7328\n\t" \
      "ds_read_b64_tr_b16 v[158:159], %[ab] offset:9120\n\t" \
      "v_mov_b32_dpp v130, v164 row_ror:8 row_mask:0xf bank_mask:0x3\n\t" \
      "v_mov_b32_dpp v131, v165 row_ror:8 row_mask:0xf bank_mask:0x3\n\t" \
      "s_waitcnt lgkmcnt(12)\n\t" \
      "v_mfma_f32_32x32x16_bf16 v[100:115], v[160:163], v[124:127], v[100:115]\n\t" \
      "ds_read_b64_tr_b16 v[160:161], %[ab] offset:7360\n\t" \
      "ds_read_b64_tr_b16 v[162:163], %[ab] offset:9152\n\t" \
      "buffer_load_dwordx4 v[124:127], %[tvo], %[srd], s76 offen sc1\n\t" \
      "s_waitcnt lgkmcnt(8)\n\t" \
      "v_mfma_f32_32x32x16_bf16 v[4:19], v[136:139], v[128:131], v[4:19]\n\t" \
      "ds_read_b64_tr_b16 v[136:137], %[ab] offset:14336\n\t" \
      "ds_read_b64_tr_b16 v[138:139], %[ab] offset:16128\n\t" \
      "s_waitcnt vmcnt(3)\n\t" \
      "v_max3_u32 v166, v132, v133, v134\n\t" \
      "v_max_u32 v166, v166, v135\n\t" \
      "v_mfma_f32_32x32x16_bf16 v[20:35], v[140:143], v[128:131], v[20:35]\n\t" \
      "ds_read_b64_tr_b16 v[140:141], %[ab] offset:14368\n\t" \
      "ds_read_b64_tr_b16 v[142:143], %[ab] offset:16160\n\t" \
      "v_cmp_eq_u32 vcc, -1, v166\n\t" \
      "v_mfma_f32_32x32x16_bf16 v[36:51], v[144:147], v[128:131], v[36:51]\n\t" \
      "ds_read_b64_tr_b16 v[144:145], %[ab] offset:14400\n\t" \
      "ds_read_b64_tr_b16 v[146:147], %[ab] offset:16192\n\t" \
      "s_cbranch_vccnz STUB9_%=\n\t" \
      "BACK9_%=:\n\t" \
      "v_permlane16_swap_b32 v132, v133\n\t" \
      "v_permlane16_swap_b32 v134, v135\n\t" \
      "s_waitcnt lgkmcnt(8)\n\t" \
      "v_mfma_f32_32x32x16_bf16 v[52:67], v[148:151], v[128:131], v[52:67]\n\t" \
      "ds_read_b64_tr_b16 v[148:149], %[ab] offset:14432\n\t" \
      "ds_read_b64_tr_b16 v[150:151], %[ab] offset:16224\n\t" \
      "v_mov_b64 v[164:165], v[132:133]\n\t" \
      "v_mfma_f32_32x32x16_bf16 v[68:83], v[152:155], v[128:131], v[68:83]\n\t" \
      "ds_read_b64_tr_b16 v[152:153], %[ab] offset:14464\n\t" \
      "ds_read_b64_tr_b16 v[154:155], %[ab] offset:16256\n\t" \
      "v_mov_b32_dpp v132, v134 row_ror:8 row_mask:0xf bank_mask:0xc\n\t" \
      "v_mov_b32_dpp v133, v135 row_ror:8 row_mask:0xf bank_mask:0xc\n\t" \
      "v_mfma_f32_32x32x16_bf16 v[84:99], v[156:159], v[128:131], v[84:99]\n\t" \
      "ds_read_b64_tr_b16 v[156:157], %[ab] offset:14496\n\t" \
      "ds_read_b64_tr_b16 v[158:159], %[ab] offset:16288\n\t" \
      "v_mov_b32_dpp v134, v164 row_ror:8 row_mask:0xf bank_mask:0x3\n\t" \
      "v_mov_b32_dpp v135, v165 row_ror:8 row_mask:0xf bank_mask:0x3\n\t" \
      "s_waitcnt lgkmcnt(12)\n\t" \
      "v_mfma_f32_32x32x16_bf16 v[100:115], v[160:163], v[128:131], v[100:115]\n\t" \
      "ds_read_b64_tr_b16 v[160:161], %[ab] offset:14528\n\t" \
      "ds_read_b64_tr_b16 v[162:163], %[ab] offset:16320\n\t" \
      "buffer_load_dwordx4 v[128:131], %[tvo], %[srd], s77 offen sc1\n\t" \
      "s_waitcnt lgkmcnt(8)\n\t" \
      "v_mfma_f32_32x32x16_bf16 v[4:19], v[136:139], v[132:135], v[4:19]\n\t" \
      "ds_read_b64_tr_b16 v[136:137], %[ab] offset:21504\n\t" \
      "ds_read_b64_tr_b16 v[138:139], %[ab] offset:23296\n\t" \
      "s_waitcnt vmcnt(3)\n\t" \
      "v_max3_u32 v166, v116, v117, v118\n\t" \
      "v_max_u32 v166, v166, v119\n\t" \
      "v_mfma_f32_32x32x16_bf16 v[20:35], v[140:143], v[132:135], v[20:35]\n\t" \
      "ds_read_b64_tr_b16 v[140:141], %[ab] offset:21536\n\t" \
      "ds_read_b64_tr_b16 v[142:143], %[ab] offset:23328\n\t" \
      "v_cmp_eq_u32 vcc, -1, v166\n\t" \
      "v_mfma_f32_32x32x16_bf16 v[36:51], v[144:147], v[132:135], v[36:51]\n\t" \
      "ds_read_b64_tr_b16 v[144:145], %[ab] offset:21568\n\t" \
      "ds_read_b64_tr_b16 v[146:147], %[ab] offset:23360\n\t" \
      "s_cbranch_vccnz STUB10_%=\n\t" \
      "BACK10_%=:\n\t" \
      "v_permlane16_swap_b32 v116, v117\n\t" \
      "v_permlane16_swap_b32 v118, v119\n\t" \
      "s_waitcnt lgkmcnt(8)\n\t" \
      "v_mfma_f32_32x32x16_bf16 v[52:67], v[148:151], v[132:135], v[52:67]\n\t" \
      "ds_read_b64_tr_b16 v[148:149], %[ab] offset:21600\n\t" \
      "ds_read_b64_tr_b16 v[150:151], %[ab] offset:23392\n\t" \
      "v_mov_b64 v[164:165], v[116:117]\n\t" \
      "v_mfma_f32_32x32x16_bf16 v[68:83], v[152:155], v[132:135], v[68:83]\n\t" \
      "ds_read_b64_tr_b16 v[152:153], %[ab] offset:21632\n\t" \
      "ds_read_b64_tr_b16 v[154:155], %[ab] offset:23424\n\t" \
      "v_mov_b32_dpp v116, v118 row_ror:8 row_mask:0xf bank_mask:0xc\n\t" \
      "v_mov_b32_dpp v117, v119 row_ror:8 row_mask:0xf bank_mask:0xc\n\t" \
      "v_mfma_f32_32x32x16_bf16 v[84:99], v[156:159], v[132:135], v[84:99]\n\t" \
      "ds_read_b64_tr_b16 v[156:157], %[ab] offset:21664\n\t" \
      "ds_read_b64_tr_b16 v[158:159], %[ab] offset:23456\n\t" \
      "v_mov_b32_dpp v118, v164 row_ror:8 row_mask:0xf bank_mask:0x3\n\t" \
      "v_mov_b32_dpp v119, v165 row_ror:8 row_mask:0xf bank_mask:0x3\n\t" \
      "s_waitcnt lgkmcnt(12)\n\t" \
      "v_mfma_f32_32x32x16_bf16 v[100:115], v[160:163], v[132:135], v[100:115]\n\t" \
      "ds_read_b64_tr_b16 v[160:161], %[ab] offset:21696\n\t" \
      "ds_read_b64_tr_b16 v[162:163], %[ab] offset:23488\n\t" \
      "buffer_load_dwordx4 v[132:135], %[tvo], %[srd], s78 offen sc1\n\t" \
      "s_waitcnt lgkmcnt(8)\n\t" \
      "v_mfma_f32_32x32x16_bf16 v[4:19], v[136:139], v[116:119], v[4:19]\n\t" \
      "ds_read_b64_tr_b16 v[136:137], %[ab] offset:28672\n\t" \
      "ds_read_b64_tr_b16 v[138:139], %[ab] offset:30464\n\t" \
      "s_waitcnt vmcnt(3)\n\t" \
      "v_max3_u32 v166, v120, v121, v122\n\t" \
      "v_max_u32 v166, v166, v123\n\t" \
      "v_mfma_f32_32x32x16_bf16 v[20:35], v[140:143], v[116:119], v[20:35]\n\t" \
      "ds_read_b64_tr_b16 v[140:141], %[ab] offset:28704\n\t" \
      "ds_read_b64_tr_b16 v[142:143], %[ab] offset:30496\n\t" \
      "v_cmp_eq_u32 vcc, -1, v166\n\t" \
      "v_mfma_f32_32x32x16_bf16 v[36:51], v[144:147], v[116:119], v[36:51]\n\t" \
      "ds_read_b64_tr_b16 v[144:145], %[ab] offset:28736\n\t" \
      "ds_read_b64_tr_b16 v[146:147], %[ab] offset:30528\n\t" \
      "s_cbranch_vccnz STUB11_%=\n\t" \
      "BACK11_%=:\n\t" \
      "v_permlane16_swap_b32 v120, v121\n\t" \
      "v_permlane16_swap_b32 v122, v123\n\t" \
      "s_waitcnt lgkmcnt(8)\n\t" \
      "v_mfma_f32_32x32x16_bf16 v[52:67], v[148:151], v[116:119], v[52:67]\n\t" \
      "ds_read_b64_tr_b16 v[148:149], %[ab] offset:28768\n\t" \
      "ds_read_b64_tr_b16 v[150:151], %[ab] offset:30560\n\t" \
      "v_mov_b64 v[164:165], v[120:121]\n\t" \
      "v_mfma_f32_32x32x16_bf16 v[68:83], v[152:155], v[116:119], v[68:83]\n\t" \
      "ds_read_b64_tr_b16 v[152:153], %[ab] offset:28800\n\t" \
      "ds_read_b64_tr_b16 v[154:155], %[ab] offset:30592\n\t" \
      "v_mov_b32_dpp v120, v122 row_ror:8 row_mask:0xf bank_mask:0xc\n\t" \
      "v_mov_b32_dpp v121, v123 row_ror:8 row_mask:0xf bank_mask:0xc\n\t" \
      "v_mfma_f32_32x32x16_bf16 v[84:99], v[156:159], v[116:119], v[84:99]\n\t" \
      "ds_read_b64_tr_b16 v[156:157], %[ab] offset:28832\n\t" \
      "ds_read_b64_tr_b16 v[158:159], %[ab] offset:30624\n\t" \
      "v_mov_b32_dpp v122, v164 row_ror:8 row_mask:0xf bank_mask:0x3\n\t" \
      "v_mov_b32_dpp v123, v165 row_ror:8 row_mask:0xf bank_mask:0x3\n\t" \
      "s_waitcnt lgkmcnt(12)\n\t" \
      "v_mfma_f32_32x32x16_bf16 v[100:115], v[160:163], v[116:119], v[100:115]\n\t" \
      "ds_read_b64_tr_b16 v[160:161], %[ab] offset:28864\n\t" \
      "ds_read_b64_tr_b16 v[162:163], %[ab] offset:30656\n\t" \
      "buffer_load_dwordx4 v[116:119], %[tvo], %[srd], s79 offen sc1\n\t" \
      "s_waitcnt lgkmcnt(8)\n\t" \
      "v_mfma_f32_32x32x16_bf16 v[4:19], v[136:139], v[120:123], v[4:19]\n\t" \
      "ds_read_b64_tr_b16 v[136:137], %[ab] offset:35840\n\t" \
      "ds_read_b64_tr_b16 v[138:139], %[ab] offset:37632\n\t" \
      "s_waitcnt vmcnt(3)\n\t" \
      "v_max3_u32 v166, v124, v125, v126\n\t" \
      "v_max_u32 v166, v166, v127\n\t" \
      "v_mfma_f32_32x32x16_bf16 v[20:35], v[140:143], v[120:123], v[20:35]\n\t" \
      "ds_read_b64_tr_b16 v[140:141], %[ab] offset:35872\n\t" \
      "ds_read_b64_tr_b16 v[142:143], %[ab] offset:37664\n\t" \
      "v_cmp_eq_u32 vcc, -1, v166\n\t" \
      "v_mfma_f32_32x32x16_bf16 v[36:51], v[144:147], v[120:123], v[36:51]\n\t" \
      "ds_read_b64_tr_b16 v[144:145], %[ab] offset:35904\n\t" \
      "ds_read_b64_tr_b16 v[146:147], %[ab] offset:37696\n\t" \
      "s_cbranch_vccnz STUB12_%=\n\t" \
      "BACK12_%=:\n\t" \
      "v_permlane16_swap_b32 v124, v125\n\t" \
      "v_permlane16_swap_b32 v126, v127\n\t" \
      "s_waitcnt lgkmcnt(8)\n\t" \
      "v_mfma_f32_32x32x16_bf16 v[52:67], v[148:151], v[120:123], v[52:67]\n\t" \
      "ds_read_b64_tr_b16 v[148:149], %[ab] offset:35936\n\t" \
      "ds_read_b64_tr_b16 v[150:151], %[ab] offset:37728\n\t" \
      "v_mov_b64 v[164:165], v[124:125]\n\t" \
      "v_mfma_f32_32x32x16_bf16 v[68:83], v[152:155], v[120:123], v[68:83]\n\t" \
      "ds_read_b64_tr_b16 v[152:153], %[ab] offset:35968\n\t" \
      "ds_read_b64_tr_b16 v[154:155], %[ab] offset:37760\n\t" \
      "v_mov_b32_dpp v124, v126 row_ror:8 row_mask:0xf bank_mask:0xc\n\t" \
      "v_mov_b32_dpp v125, v127 row_ror:8 row_mask:0xf bank_mask:0xc\n\t" \
      "v_mfma_f32_32x32x16_bf16 v[84:99], v[156:159], v[120:123], v[84:99]\n\t" \
      "ds_read_b64_tr_b16 v[156:157], %[ab] offset:36000\n\t" \
      "ds_read_b64_tr_b16 v[158:159], %[ab] offset:37792\n\t" \
      "v_mov_b32_dpp v126, v164 row_ror:8 row_mask:0xf bank_mask:0x3\n\t" \
      "v_mov_b32_dpp v127, v165 row_ror:8 row_mask:0xf bank_mask:0x3\n\t" \
      "s_waitcnt lgkmcnt(12)\n\t" \
      "v_mfma_f32_32x32x16_bf16 v[100:115], v[160:163], v[120:123], v[100:115]\n\t" \
      "ds_read_b64_tr_b16 v[160:161], %[ab] offset:36032\n\t" \
      "ds_read_b64_tr_b16 v[162:163], %[ab] offset:37824\n\t" \
      "buffer_load_dwordx4 v[120:123], %[tvo], %[srd], s80 offen sc1\n\t" \
      "s_waitcnt lgkmcnt(8)\n\t" \
      "v_mfma_f32_32x32x16_bf16 v[4:19], v[136:139], v[124:127], v[4:19]\n\t" \
      "ds_read_b64_tr_b16 v[136:137], %[ab] offset:43008\n\t" \
      "ds_read_b64_tr_b16 v[138:139], %[ab] offset:44800\n\t" \
      "s_waitcnt vmcnt(3)\n\t" \
      "v_max3_u32 v166, v128, v129, v130\n\t" \
      "v_max_u32 v166, v166, v131\n\t" \
      "v_mfma_f32_32x32x16_bf16 v[20:35], v[140:143], v[124:127], v[20:35]\n\t" \
      "ds_read_b64_tr_b16 v[140:141], %[ab] offset:43040\n\t" \
      "ds_read_b64_tr_b16 v[142:143], %[ab] offset:44832\n\t" \
      "v_cmp_eq_u32 vcc, -1, v166\n\t" \
      "v_mfma_f32_32x32x16_bf16 v[36:51], v[144:147], v[124:127], v[36:51]\n\t" \
      "ds_read_b64_tr_b16 v[144:145], %[ab] offset:43072\n\t" \
      "ds_read_b64_tr_b16 v[146:147], %[ab] offset:44864\n\t" \
      "s_cbranch_vccnz STUB13_%=\n\t" \
      "BACK13_%=:\n\t" \
      "v_permlane16_swap_b32 v128, v129\n\t" \
      "v_permlane16_swap_b32 v130, v131\n\t" \
      "s_waitcnt lgkmcnt(8)\n\t" \
      "v_mfma_f32_32x32x16_bf16 v[52:67], v[148:151], v[124:127], v[52:67]\n\t" \
      "ds_read_b64_tr_b16 v[148:149], %[ab] offset:43104\n\t" \
      "ds_read_b64_tr_b16 v[150:151], %[ab] offset:44896\n\t" \
      "v_mov_b64 v[164:165], v[128:129]\n\t" \
      "v_mfma_f32_32x32x16_bf16 v[68:83], v[152:155], v[124:127], v[68:83]\n\t" \
      "ds_read_b64_tr_b16 v[152:153], %[ab] offset:43136\n\t" \
      "ds_read_b64_tr_b16 v[154:155], %[ab] offset:44928\n\t" \
      "v_mov_b32_dpp v128, v130 row_ror:8 row_mask:0xf bank_mask:0xc\n\t" \
      "v_mov_b32_dpp v129, v131 row_ror:8 row_mask:0xf bank_mask:0xc\n\t" \
      "v_mfma_f32_32x32x16_bf16 v[84:99], v[156:159], v[124:127], v[84:99]\n\t" \
      "ds_read_b64_tr_b16 v[156:157], %[ab] offset:43168\n\t" \
      "ds_read_b64_tr_b16 v[158:159], %[ab] offset:44960\n\t" \
      "v_mov_b32_dpp v130, v164 row_ror:8 row_mask:0xf bank_mask:0x3\n\t" \
      "v_mov_b32_dpp v131, v165 row_ror:8 row_mask:0xf bank_mask:0x3\n\t" \
      "s_waitcnt lgkmcnt(12)\n\t" \
      "v_mfma_f32_32x32x16_bf16 v[100:115], v[160:163], v[124:127], v[100:115]\n\t" \
      "ds_read_b64_tr_b16 v[160:161], %[ab] offset:43200\n\t" \
      "ds_read_b64_tr_b16 v[162:163], %[ab] offset:44992\n\t" \
      "buffer_load_dwordx4 v[124:127], %[tvo], %[srd], s81 offen sc1\n\t" \
      "s_waitcnt lgkmcnt(8)\n\t" \
      "v_mfma_f32_32x32x16_bf16 v[4:19], v[136:139], v[128:131], v[4:19]\n\t" \
      "s_waitcnt lgkmcnt(0)\n\t" \
      "s_barrier\n\t" \
      "v_add_u32 %[ab], 50176, %[ab]\n\t" \
      "ds_read_b64_tr_b16 v[136:137], %[ab] offset:0\n\t" \
      "ds_read_b64_tr_b16 v[138:139], %[ab] offset:1792\n\t" \
      "s_waitcnt vmcnt(3)\n\t" \
      "v_max3_u32 v166, v132, v133, v134\n\t" \
      "v_max_u32 v166, v166, v135\n\t" \
      "v_mfma_f32_32x32x16_bf16 v[20:35], v[140:143], v[128:131], v[20:35]\n\t" \
      "ds_read_b64_tr_b16 v[140:141], %[ab] offset:32\n\t" \
      "ds_read_b64_tr_b16 v[142:143], %[ab] offset:1824\n\t" \
      "v_cmp_eq_u32 vcc, -1, v166\n\t" \
      "v_mfma_f32_32x32x16_bf16 v[36:51], v[144:147], v[128:131], v[36:51]\n\t" \
      "ds_read_b64_tr_b16 v[144:145], %[ab] offset:64\n\t" \
      "ds_read_b64_tr_b16 v[146:147], %[ab] offset:1856\n\t" \
      "s_cbranch_vccnz STUB14_%=\n\t" \
      "BACK14_%=:\n\t" \
      "v_permlane16_swap_b32 v132, v133\n\t" \
      "v_permlane16_swap_b32 v134, v135\n\t" \
      "v_mfma_f32_32x32x16_bf16 v[52:67], v[148:151], v[128:131], v[52:67]\n\t" \
      "ds_read_b64_tr_b16 v[148:149], %[ab] offset:96\n\t" \
      "ds_read_b64_tr_b16 v[150:151], %[ab] offset:1888\n\t" \
      "v_mov_b64 v[164:165], v[132:133]\n\t" \
      "v_mfma_f32_32x32x16_bf16 v[68:83], v[152:155], v[128:131], v[68:83]\n\t" \
      "ds_read_b64_tr_b16 v[152:153], %[ab] offset:128\n\t" \
      "ds_read_b64_tr_b16 v[154:155], %[ab] offset:1920\n\t" \
      "v_mov_b32_dpp v132, v134 row_ror:8 row_mask:0xf bank_mask:0xc\n\t" \
      "v_mov_b32_dpp v133, v135 row_ror:8 row_mask:0xf bank_mask:0xc\n\t" \
      "v_mfma_f32_32x32x16_bf16 v[84:99], v[156:159], v[128:131], v[84:99]\n\t" \
      "ds_read_b64_tr_b16 v[156:157], %[ab] offset:160\n\t" \
      "ds_read_b64_tr_b16 v[158:159], %[ab] offset:1952\n\t" \
      "v_mov_b32_dpp v134, v164 row_ror:8 row_mask:0xf bank_mask:0x3\n\t" \
      "v_mov_b32_dpp v135, v165 row_ror:8 row_mask:0xf bank_mask:0x3\n\t" \
      "v_mfma_f32_32x32x16_bf16 v[100:115], v[160:163], v[128:131], v[100:115]\n\t" \
      "ds_read_b64_tr_b16 v[160:161], %[ab] offset:192\n\t" \
      "ds_read_b64_tr_b16 v[162:163], %[ab] offset:1984\n\t" \
      "buffer_load_dwordx4 v[128:131], %[tvo], %[srd], s82 offen sc1\n\t" \
      "s_waitcnt lgkmcnt(8)\n\t" \
      "v_mfma_f32_32x32x16_bf16 v[4:19], v[136:139], v[132:135], v[4:19]\n\t" \
      "ds_read_b64_tr_b16 v[136:137], %[ab] offset:7168\n\t" \
      "ds_read_b64_tr_b16 v[138:139], %[ab] offset:8960\n\t" \
      "s_waitcnt vmcnt(3)\n\t" \
      "v_max3_u32 v166, v116, v117, v118\n\t" \
      "v_max_u32 v166, v166, v119\n\t" \
      "v_mfma_f32_32x32x16_bf16 v[20:35], v[140:143], v[132:135], v[20:35]\n\t" \
      "ds_read_b64_tr_b16 v[140:141], %[ab] offset:7200\n\t" \
      "ds_read_b64_tr_b16 v[142:143], %[ab] offset:8992\n\t" \
      "v_cmp_eq_u32 vcc, -1, v166\n\t" \
      "v_mfma_f32_32x32x16_bf16 v[36:51], v[144:147], v[132:135], v[36:51]\n\t" \
      "ds_read_b64_tr_b16 v[144:145], %[ab] offset:7232\n\t" \
      "ds_read_b64_tr_b16 v[146:147], %[ab] offset:9024\n\t" \
      "s_cbranch_vccnz STUB15_%=\n\t" \
      "BACK15_%=:\n\t" \
      "v_permlane16_swap_b32 v116, v117\n\t" \
      "v_permlane16_swap_b32 v118, v119\n\t" \
      "s_waitcnt lgkmcnt(8)\n\t" \
      "v_mfma_f32_32x32x16_bf16 v[52:67], v[148:151], v[132:135], v[52:67]\n\t" \
      "ds_read_b64_tr_b16 v[148:149], %[ab] offset:7264\n\t" \
      "ds_read_b64_tr_b16 v[150:151], %[ab] offset:9056\n\t" \
      "v_mov_b64 v[164:165], v[116:117]\n\t" \
      "v_mfma_f32_32x32x16_bf16 v[68:83], v[152:155], v[132:135], v[68:83]\n\t" \
      "ds_read_b64_tr_b16 v[152:153], %[ab] offset:7296\n\t" \
      "ds_read_b64_tr_b16 v[154:155], %[ab] offset:9088\n\t" \
      "v_mov_b32_dpp v116, v118 row_ror:8 row_mask:0xf bank_mask:0xc\n\t" \
      "v_mov_b32_dpp v117, v119 row_ror:8 row_mask:0xf bank_mask:0xc\n\t" \
      "v_mfma_f32_32x32x16_bf16 v[84:99], v[156:159], v[132:135], v[84:99]\n\t" \
      "ds_read_b64_tr_b16 v[156:157], %[ab] offset:7328\n\t" \
      "ds_read_b64_tr_b16 v[158:159], %[ab] offset:9120\n\t" \
      "v_mov_b32_dpp v118, v164 row_ror:8 row_mask:0xf bank_mask:0x3\n\t" \
      "v_mov_b32_dpp v119, v165 row_ror:8 row_mask:0xf bank_mask:0x3\n\t" \
      "s_waitcnt lgkmcnt(12)\n\t" \
      "v_mfma_f32_32x32x16_bf16 v[100:115], v[160:163], v[132:135], v[100:115]\n\t" \
      "ds_read_b64_tr_b16 v[160:161], %[ab] offset:7360\n\t" \
      "ds_read_b64_tr_b16 v[162:163], %[ab] offset:9152\n\t" \
      "buffer_load_dwordx4 v[132:135], %[tvo], %[srd], s83 offen sc1\n\t" \
      "s_waitcnt lgkmcnt(8)\n\t" \
      "v_mfma_f32_32x32x16_bf16 v[4:19], v[136:139], v[116:119], v[4:19]\n\t" \
      "ds_read_b64_tr_b16 v[136:137], %[ab] offset:14336\n\t" \
      "ds_read_b64_tr_b16 v[138:139], %[ab] offset:16128\n\t" \
      "s_waitcnt vmcnt(3)\n\t" \
      "v_max3_u32 v166, v120, v121, v122\n\t" \
      "v_max_u32 v166, v166, v123\n\t" \
      "v_mfma_f32_32x32x16_bf16 v[20:35], v[140:143], v[116:119], v[20:35]\n\t" \
      "ds_read_b64_tr_b16 v[140:141], %[ab] offset:14368\n\t" \
      "ds_read_b64_tr_b16 v[142:143], %[ab] offset:16160\n\t" \
      "v_cmp_eq_u32 vcc, -1, v166\n\t" \
      "v_mfma_f32_32x32x16_bf16 v[36:51], v[144:147], v[116:119], v[36:51]\n\t" \
      "ds_read_b64_tr_b16 v[144:145], %[ab] offset:14400\n\t" \
      "ds_read_b64_tr_b16 v[146:147], %[ab] offset:16192\n\t" \
      "s_cbranch_vccnz STUB16_%=\n\t" \
      "BACK16_%=:\n\t" \
      "v_permlane16_swap_b32 v120, v121\n\t" \
      "v_permlane16_swap_b32 v122, v123\n\t" \
      "s_waitcnt lgkmcnt(8)\n\t" \
      "v_mfma_f32_32x32x16_bf16 v[52:67], v[148:151], v[116:119], v[52:67]\n\t" \
      "ds_read_b64_tr_b16 v[148:149], %[ab] offset:14432\n\t" \
      "ds_read_b64_tr_b16 v[150:151], %[ab] offset:16224\n\t" \
      "v_mov_b64 v[164:165], v[120:121]\n\t" \
      "v_mfma_f32_32x32x16_bf16 v[68:83], v[152:155], v[116:119], v[68:83]\n\t" \
      "ds_read_b64_tr_b16 v[152:153], %[ab] offset:14464\n\t" \
      "ds_read_b64_tr_b16 v[154:155], %[ab] offset:16256\n\t" \
      "v_mov_b32_dpp v120, v122 row_ror:8 row_mask:0xf bank_mask:0xc\n\t" \
      "v_mov_b32_dpp v121, v123 row_ror:8 row_mask:0xf bank_mask:0xc\n\t" \
      "v_mfma_f32_32x32x16_bf16 v[84:99], v[156:159], v[116:119], v[84:99]\n\t" \
      "ds_read_b64_tr_b16 v[156:157], %[ab] offset:14496\n\t" \
      "ds_read_b64_tr_b16 v[158:159], %[ab] offset:16288\n\t" \
      "v_mov_b32_dpp v122, v164 row_ror:8 row_mask:0xf bank_mask:0x3\n\t" \
      "v_mov_b32_dpp v123, v165 row_ror:8 row_mask:0xf bank_mask:0x3\n\t" \
      "s_waitcnt lgkmcnt(12)\n\t" \
      "v_mfma_f32_32x32x16_bf16 v[100:115], v[160:163], v[116:119], v[100:115]\n\t" \
      "ds_read_b64_tr_b16 v[160:161], %[ab] offset:14528\n\t" \
      "ds_read_b64_tr_b16 v[162:163], %[ab] offset:16320\n\t" \
      "buffer_load_dwordx4 v[116:119], %[tvo], %[srd], s84 offen sc1\n\t" \
      "s_waitcnt lgkmcnt(8)\n\t" \
      "v_mfma_f32_32x32x16_bf16 v[4:19], v[136:139], v[120:123], v[4:19]\n\t" \
      "ds_read_b64_tr_b16 v[136:137], %[ab] offset:21504\n\t" \
      "ds_read_b64_tr_b16 v[138:139], %[ab] offset:23296\n\t" \
      "s_waitcnt vmcnt(3)\n\t" \
      "v_max3_u32 v166, v124, v125, v126\n\t" \
      "v_max_u32 v166, v166, v127\n\t" \
      "v_mfma_f32_32x32x16_bf16 v[20:35], v[140:143], v[120:123], v[20:35]\n\t" \
      "ds_read_b64_tr_b16 v[140:141], %[ab] offset:21536\n\t" \
      "ds_read_b64_tr_b16 v[142:143], %[ab] offset:23328\n\t" \
      "v_cmp_eq_u32 vcc, -1, v166\n\t" \
      "v_mfma_f32_32x32x16_bf16 v[36:51], v[144:147], v[120:123], v[36:51]\n\t" \
      "ds_read_b64_tr_b16 v[144:145], %[ab] offset:21568\n\t" \
      "ds_read_b64_tr_b16 v[146:147], %[ab] offset:23360\n\t" \
      "s_cbranch_vccnz STUB17_%=\n\t" \
      "BACK17_%=:\n\t" \
      "v_permlane16_swap_b32 v124, v125\n\t" \
      "v_permlane16_swap_b32 v126, v127\n\t" \
      "s_waitcnt lgkmcnt(8)\n\t" \
      "v_mfma_f32_32x32x16_bf16 v[52:67], v[148:151], v[120:123], v[52:67]\n\t" \
      "ds_read_b64_tr_b16 v[148:149], %[ab] offset:21600\n\t" \
      "ds_read_b64_tr_b16 v[150:151], %[ab] offset:23392\n\t" \
      "v_mov_b64 v[164:165], v[124:125]\n\t" \
      "v_mfma_f32_32x32x16_bf16 v[68:83], v[152:155], v[120:123], v[68:83]\n\t" \
      "ds_read_b64_tr_b16 v[152:153], %[ab] offset:21632\n\t" \
      "ds_read_b64_tr_b16 v[154:155], %[ab] offset:23424\n\t" \
      "v_mov_b32_dpp v124, v126 row_ror:8 row_mask:0xf bank_mask:0xc\n\t" \
      "v_mov_b32_dpp v125, v127 row_ror:8 row_mask:0xf bank_mask:0xc\n\t" \
      "v_mfma_f32_32x32x16_bf16 v[84:99], v[156:159], v[120:123], v[84:99]\n\t" \
      "ds_read_b64_tr_b16 v[156:157], %[ab] offset:21664\n\t" \
      "ds_read_b64_tr_b16 v[158:159], %[ab] offset:23456\n\t" \
      "v_mov_b32_dpp v126, v164 row_ror:8 row_mask:0xf bank_mask:0x3\n\t" \
      "v_mov_b32_dpp v127, v165 row_ror:8 row_mask:0xf bank_mask:0x3\n\t" \
      "s_waitcnt lgkmcnt(12)\n\t" \
      "v_mfma_f32_32x32x16_bf16 v[100:115], v[160:163], v[120:123], v[100:115]\n\t" \
      "ds_read_b64_tr_b16 v[160:161], %[ab] offset:21696\n\t" \
      "ds_read_b64_tr_b16 v[162:163], %[ab] offset:23488\n\t" \
      "buffer_load_dwordx4 v[120:123], %[tvo], %[srd], s85 offen sc1\n\t" \
      "s_waitcnt lgkmcnt(8)\n\t" \
      "v_mfma_f32_32x32x16_bf16 v[4:19], v[136:139], v[124:127], v[4:19]\n\t" \
      "ds_read_b64_tr_b16 v[136:137], %[ab] offset:28672\n\t" \
      "ds_read_b64_tr_b16 v[138:139], %[ab] offset:30464\n\t" \
      "s_waitcnt vmcnt(3)\n\t" \
      "v_max3_u32 v166, v128, v129, v130\n\t" \
      "v_max_u32 v166, v166, v131\n\t" \
      "v_mfma_f32_32x32x16_bf16 v[20:35], v[140:143], v[124:127], v[20:35]\n\t" \
      "ds_read_b64_tr_b16 v[140:141], %[ab] offset:28704\n\t" \
      "ds_read_b64_tr_b16 v[142:143], %[ab] offset:30496\n\t" \
      "v_cmp_eq_u32 vcc, -1, v166\n\t" \
      "v_mfma_f32_32x32x16_bf16 v[36:51], v[144:147], v[124:127], v[36:51]\n\t" \
      "ds_read_b64_tr_b16 v[144:145], %[ab] offset:28736\n\t" \
      "ds_read_b64_tr_b16 v[146:147], %[ab] offset:30528\n\t" \
      "s_cbranch_vccnz STUB18_%=\n\t" \
      "BACK18_%=:\n\t" \
      "v_permlane16_swap_b32 v128, v129\n\t" \
      "v_permlane16_swap_b32 v130, v131\n\t" \
      "s_waitcnt lgkmcnt(8)\n\t" \
      "v_mfma_f32_32x32x16_bf16 v[52:67], v[148:151], v[124:127], v[52:67]\n\t" \
      "ds_read_b64_tr_b16 v[148:149], %[ab] offset:28768\n\t" \
      "ds_read_b64_tr_b16 v[150:151], %[ab] offset:30560\n\t" \
      "v_mov_b64 v[164:165], v[128:129]\n\t" \
      "v_mfma_f32_32x32x16_bf16 v[68:83], v[152:155], v[124:127], v[68:83]\n\t" \
      "ds_read_b64_tr_b16 v[152:153], %[ab] offset:28800\n\t" \
      "ds_read_b64_tr_b16 v[154:155], %[ab] offset:30592\n\t" \
      "v_mov_b32_dpp v128, v130 row_ror:8 row_mask:0xf bank_mask:0xc\n\t" \
      "v_mov_b32_dpp v129, v131 row_ror:8 row_mask:0xf bank_mask:0xc\n\t" \
      "v_mfma_f32_32x32x16_bf16 v[84:99], v[156:159], v[124:127], v[84:99]\n\t" \
      "ds_read_b64_tr_b16 v[156:157], %[ab] offset:28832\n\t" \
      "ds_read_b64_tr_b16 v[158:159], %[ab] offset:30624\n\t" \
      "v_mov_b32_dpp v130, v164 row_ror:8 row_mask:0xf bank_mask:0x3\n\t" \
      "v_mov_b32_dpp v131, v165 row_ror:8 row_mask:0xf bank_mask:0x3\n\t" \
      "s_waitcnt lgkmcnt(12)\n\t" \
      "v_mfma_f32_32x32x16_bf16 v[100:115], v[160:163], v[124:127], v[100:115]\n\t" \
      "ds_read_b64_tr_b16 v[160:161], %[ab] offset:28864\n\t" \
      "ds_read_b64_tr_b16 v[162:163], %[ab] offset:30656\n\t" \
      "buffer_load_dwordx4 v[124:127], %[tvo], %[srd], s86 offen sc1\n\t" \
      "s_waitcnt lgkmcnt(8)\n\t" \
      "v_mfma_f32_32x32x16_bf16 v[4:19], v[136:139], v[128:131], v[4:19]\n\t" \
      "ds_read_b64_tr_b16 v[136:137], %[ab] offset:35840\n\t" \
      "ds_read_b64_tr_b16 v[138:139], %[ab] offset:37632\n\t" \
      "s_waitcnt vmcnt(3)\n\t" \
      "v_max3_u32 v166, v132, v133, v134\n\t" \
      "v_max_u32 v166, v166, v135\n\t" \
      "v_mfma_f32_32x32x16_bf16 v[20:35], v[140:143], v[128:131], v[20:35]\n\t" \
      "ds_read_b64_tr_b16 v[140:141], %[ab] offset:35872\n\t" \
      "ds_read_b64_tr_b16 v[142:143], %[ab] offset:37664\n\t" \
      "v_cmp_eq_u32 vcc, -1, v166\n\t" \
      "v_mfma_f32_32x32x16_bf16 v[36:51], v[144:147], v[128:131], v[36:51]\n\t" \
      "ds_read_b64_tr_b16 v[144:145], %[ab] offset:35904\n\t" \
      "ds_read_b64_tr_b16 v[146:147], %[ab] offset:37696\n\t" \
      "s_cbranch_vccnz STUB19_%=\n\t" \
      "BACK19_%=:\n\t" \
      "v_permlane16_swap_b32 v132, v133\n\t" \
      "v_permlane16_swap_b32 v134, v135\n\t" \
      "s_waitcnt lgkmcnt(8)\n\t" \
      "v_mfma_f32_32x32x16_bf16 v[52:67], v[148:151], v[128:131], v[52:67]\n\t" \
      "ds_read_b64_tr_b16 v[148:149], %[ab] offset:35936\n\t" \
      "ds_read_b64_tr_b16 v[150:151], %[ab] offset:37728\n\t" \
      "v_mov_b64 v[164:165], v[132:133]\n\t" \
      "v_mfma_f32_32x32x16_bf16 v[68:83], v[152:155], v[128:131], v[68:83]\n\t" \
      "ds_read_b64_tr_b16 v[152:153], %[ab] offset:35968\n\t" \
      "ds_read_b64_tr_b16 v[154:155], %[ab] offset:37760\n\t" \
      "v_mov_b32_dpp v132, v134 row_ror:8 row_mask:0xf bank_mask:0xc\n\t" \
      "v_mov_b32_dpp v133, v135 row_ror:8 row_mask:0xf bank_mask:0xc\n\t" \
      "v_mfma_f32_32x32x16_bf16 v[84:99], v[156:159], v[128:131], v[84:99]\n\t" \
      "ds_read_b64_tr_b16 v[156:157], %[ab] offset:36000\n\t" \
      "ds_read_b64_tr_b16 v[158:159], %[ab] offset:37792\n\t" \
      "v_mov_b32_dpp v134, v164 row_ror:8 row_mask:0xf bank_mask:0x3\n\t" \
      "v_mov_b32_dpp v135, v165 row_ror:8 row_mask:0xf bank_mask:0x3\n\t" \
      "s_waitcnt lgkmcnt(12)\n\t" \
      "v_mfma_f32_32x32x16_bf16 v[100:115], v[160:163], v[128:131], v[100:115]\n\t" \
      "ds_read_b64_tr_b16 v[160:161], %[ab] offset:36032\n\t" \
      "ds_read_b64_tr_b16 v[162:163], %[ab] offset:37824\n\t" \
      "buffer_load_dwordx4 v[128:131], %[tvo], %[srd], s87 offen sc1\n\t" \
      "s_waitcnt lgkmcnt(8)\n\t" \
      "v_mfma_f32_32x32x16_bf16 v[4:19], v[136:139], v[132:135], v[4:19]\n\t" \
      "ds_read_b64_tr_b16 v[136:137], %[ab] offset:43008\n\t" \
      "ds_read_b64_tr_b16 v[138:139], %[ab] offset:44800\n\t" \
      "s_waitcnt vmcnt(3)\n\t" \
      "v_max3_u32 v166, v116, v117, v118\n\t" \
      "v_max_u32 v166, v166, v119\n\t" \
      "v_mfma_f32_32x32x16_bf16 v[20:35], v[140:143], v[132:135], v[20:35]\n\t" \
      "ds_read_b64_tr_b16 v[140:141], %[ab] offset:43040\n\t" \
      "ds_read_b64_tr_b16 v[142:143], %[ab] offset:44832\n\t" \
      "v_cmp_eq_u32 vcc, -1, v166\n\t" \
      "v_mfma_f32_32x32x16_bf16 v[36:51], v[144:147], v[132:135], v[36:51]\n\t" \
      "ds_read_b64_tr_b16 v[144:145], %[ab] offset:43072\n\t" \
      "ds_read_b64_tr_b16 v[146:147], %[ab] offset:44864\n\t" \
      "s_cbranch_vccnz STUB20_%=\n\t" \
      "BACK20_%=:\n\t" \
      "v_permlane16_swap_b32 v116, v117\n\t" \
      "v_permlane16_swap_b32 v118, v119\n\t" \
      "s_waitcnt lgkmcnt(8)\n\t" \
      "v_mfma_f32_32x32x16_bf16 v[52:67], v[148:151], v[132:135], v[52:67]\n\t" \
      "ds_read_b64_tr_b16 v[148:149], %[ab] offset:43104\n\t" \
      "ds_read_b64_tr_b16 v[150:151], %[ab] offset:44896\n\t" \
      "v_mov_b64 v[164:165], v[116:117]\n\t" \
      "v_mfma_f32_32x32x16_bf16 v[68:83], v[152:155], v[132:135], v[68:83]\n\t" \
      "ds_read_b64_tr_b16 v[152:153], %[ab] offset:43136\n\t" \
      "ds_read_b64_tr_b16 v[154:155], %[ab] offset:44928\n\t" \
      "v_mov_b32_dpp v116, v118 row_ror:8 row_mask:0xf bank_mask:0xc\n\t" \
      "v_mov_b32_dpp v117, v119 row_ror:8 row_mask:0xf bank_mask:0xc\n\t" \
      "v_mfma_f32_32x32x16_bf16 v[84:99], v[156:159], v[132:135], v[84:99]\n\t" \
      "ds_read_b64_tr_b16 v[156:157], %[ab] offset:43168\n\t" \
      "ds_read_b64_tr_b16 v[158:159], %[ab] offset:44960\n\t" \
      "v_mov_b32_dpp v118, v164 row_ror:8 row_mask:0xf bank_mask:0x3\n\t" \
      "v_mov_b32_dpp v119, v165 row_ror:8 row_mask:0xf bank_mask:0x3\n\t" \
      "s_waitcnt lgkmcnt(12)\n\t" \
      "v_mfma_f32_32x32x16_bf16 v[100:115], v[160:163], v[132:135], v[100:115]\n\t" \
      "ds_read_b64_tr_b16 v[160:161], %[ab] offset:43200\n\t" \
      "ds_read_b64_tr_b16 v[162:163], %[ab] offset:44992\n\t" \
      "buffer_load_dwordx4 v[132:135], %[tvo], %[srd], s88 offen sc1\n\t" \
      "s_waitcnt lgkmcnt(8)\n\t" \
      "v_mfma_f32_32x32x16_bf16 v[4:19], v[136:139], v[116:119], v[4:19]\n\t" \
      "s_waitcnt lgkmcnt(0)\n\t" \
      "s_barrier\n\t" \
      "v_subrev_u32 %[ab], 100352, %[ab]\n\t" \
      "ds_read_b64_tr_b16 v[136:137], %[ab] offset:0\n\t" \
      "ds_read_b64_tr_b16 v[138:139], %[ab] offset:1792\n\t" \
      "s_waitcnt vmcnt(3)\n\t" \
      "v_max3_u32 v166, v120, v121, v122\n\t" \
      "v_max_u32 v166, v166, v123\n\t" \
      "v_mfma_f32_32x32x16_bf16 v[20:35], v[140:143], v[116:119], v[20:35]\n\t" \
      "ds_read_b64_tr_b16 v[140:141], %[ab] offset:32\n\t" \
      "ds_read_b64_tr_b16 v[142:143], %[ab] offset:1824\n\t" \
      "v_cmp_eq_u32 vcc, -1, v166\n\t" \
      "v_mfma_f32_32x32x16_bf16 v[36:51], v[144:147], v[116:119], v[36:51]\n\t" \
      "ds_read_b64_tr_b16 v[144:145], %[ab] offset:64\n\t" \
      "ds_read_b64_tr_b16 v[146:147], %[ab] offset:1856\n\t" \
      "s_cbranch_vccnz STUB21_%=\n\t" \
      "BACK21_%=:\n\t" \
      "v_permlane16_swap_b32 v120, v121\n\t" \
      "v_permlane16_swap_b32 v122, v123\n\t" \
      "v_mfma_f32_32x32x16_bf16 v[52:67], v[148:151], v[116:119], v[52:67]\n\t" \
      "ds_read_b64_tr_b16 v[148:149], %[ab] offset:96\n\t" \
      "ds_read_b64_tr_b16 v[150:151], %[ab] offset:1888\n\t" \
      "v_mov_b64 v[164:165], v[120:121]\n\t" \
      "v_mfma_f32_32x32x16_bf16 v[68:83], v[152:155], v[116:119], v[68:83]\n\t" \
      "ds_read_b64_tr_b16 v[152:153], %[ab] offset:128\n\t" \
      "ds_read_b64_tr_b16 v[154:155], %[ab] offset:1920\n\t" \
      "v_mov_b32_dpp v120, v122 row_ror:8 row_mask:0xf bank_mask:0xc\n\t" \
      "v_mov_b32_dpp v121, v123 row_ror:8 row_mask:0xf bank_mask:0xc\n\t" \
      "v_mfma_f32_32x32x16_bf16 v[84:99], v[156:159], v[116:119], v[84:99]\n\t" \
      "ds_read_b64_tr_b16 v[156:157], %[ab] offset:160\n\t" \
      "ds_read_b64_tr_b16 v[158:159], %[ab] offset:1952\n\t" \
      "v_mov_b32_dpp v122, v164 row_ror:8 row_mask:0xf bank_mask:0x3\n\t" \
      "v_mov_b32_dpp v123, v165 row_ror:8 row_mask:0xf bank_mask:0x3\n\t" \
      "v_mfma_f32_32x32x16_bf16 v[100:115], v[160:163], v[116:119], v[100:115]\n\t" \
      "ds_read_b64_tr_b16 v[160:161], %[ab] offset:192\n\t" \
      "ds_read_b64_tr_b16 v[162:163], %[ab] offset:1984\n\t" \
      "buffer_load_dwordx4 v[116:119], %[tvo], %[srd], s89 offen sc1\n\t" \
      "s_waitcnt lgkmcnt(8)\n\t" \
      "v_mfma_f32_32x32x16_bf16 v[4:19], v[136:139], v[120:123], v[4:19]\n\t" \
      "ds_read_b64_tr_b16 v[136:137], %[ab] offset:7168\n\t" \
      "ds_read_b64_tr_b16 v[138:139], %[ab] offset:8960\n\t" \
      "s_waitcnt vmcnt(3)\n\t" \
      "v_max3_u32 v166, v124, v125, v126\n\t" \
      "v_max_u32 v166, v166, v127\n\t" \
      "v_mfma_f32_32x32x16_bf16 v[20:35], v[140:143], v[120:123], v[20:35]\n\t" \
      "ds_read_b64_tr_b16 v[140:141], %[ab] offset:7200\n\t" \
      "ds_read_b64_tr_b16 v[142:143], %[ab] offset:8992\n\t" \
      "v_cmp_eq_u32 vcc, -1, v166\n\t" \
      "v_mfma_f32_32x32x16_bf16 v[36:51], v[144:147], v[120:123], v[36:51]\n\t" \
      "ds_read_b64_tr_b16 v[144:145], %[ab] offset:7232\n\t" \
      "ds_read_b64_tr_b16 v[146:147], %[ab] offset:9024\n\t" \
      "s_cbranch_vccnz STUB22_%=\n\t" \
      "BACK22_%=:\n\t" \
      "v_permlane16_swap_b32 v124, v125\n\t" \
      "v_permlane16_swap_b32 v126, v127\n\t" \
      "s_waitcnt lgkmcnt(8)\n\t" \
      "v_mfma_f32_32x32x16_bf16 v[52:67], v[148:151], v[120:123], v[52:67]\n\t" \
      "ds_read_b64_tr_b16 v[148:149], %[ab] offset:7264\n\t" \
      "ds_read_b64_tr_b16 v[150:151], %[ab] offset:9056\n\t" \
      "v_mov_b64 v[164:165], v[124:125]\n\t" \
      "v_mfma_f32_32x32x16_bf16 v[68:83], v[152:155], v[120:123], v[68:83]\n\t" \
      "ds_read_b64_tr_b16 v[152:153], %[ab] offset:7296\n\t" \
      "ds_read_b64_tr_b16 v[154:155], %[ab] offset:9088\n\t" \
      "v_mov_b32_dpp v124, v126 row_ror:8 row_mask:0xf bank_mask:0xc\n\t" \
      "v_mov_b32_dpp v125, v127 row_ror:8 row_mask:0xf bank_mask:0xc\n\t" \
      "v_mfma_f32_32x32x16_bf16 v[84:99], v[156:159], v[120:123], v[84:99]\n\t" \
      "ds_read_b64_tr_b16 v[156:157], %[ab] offset:7328\n\t" \
      "ds_read_b64_tr_b16 v[158:159], %[ab] offset:9120\n\t" \
      "v_mov_b32_dpp v126, v164 row_ror:8 row_mask:0xf bank_mask:0x3\n\t" \
      "v_mov_b32_dpp v127, v165 row_ror:8 row_mask:0xf bank_mask:0x3\n\t" \
      "s_waitcnt lgkmcnt(12)\n\t" \
      "v_mfma_f32_32x32x16_bf16 v[100:115], v[160:163], v[120:123], v[100:115]\n\t" \
      "ds_read_b64_tr_b16 v[160:161], %[ab] offset:7360\n\t" \
      "ds_read_b64_tr_b16 v[162:163], %[ab] offset:9152\n\t" \
      "buffer_load_dwordx4 v[120:123], %[tvo], %[srd], s90 offen sc1\n\t" \
      "s_waitcnt lgkmcnt(8)\n\t" \
      "v_mfma_f32_32x32x16_bf16 v[4:19], v[136:139], v[124:127], v[4:19]\n\t" \
      "ds_read_b64_tr_b16 v[136:137], %[ab] offset:14336\n\t" \
      "ds_read_b64_tr_b16 v[138:139], %[ab] offset:16128\n\t" \
      "s_waitcnt vmcnt(3)\n\t" \
      "v_max3_u32 v166, v128, v129, v130\n\t" \
      "v_max_u32 v166, v166, v131\n\t" \
      "v_cmp_eq_u32 vcc, -1, v166\n\t" \
      "v_mfma_f32_32x32x16_bf16 v[20:35], v[140:143], v[124:127], v[20:35]\n\t" \
      "ds_read_b64_tr_b16 v[140:141], %[ab] offset:14368\n\t" \
      "ds_read_b64_tr_b16 v[142:143], %[ab] offset:16160\n\t" \
      "s_cbranch_vccnz STUB23_%=\n\t" \
      "BACK23_%=:\n\t" \
      "v_permlane16_swap_b32 v128, v129\n\t" \
      "v_permlane16_swap_b32 v130, v131\n\t" \
      "v_mfma_f32_32x32x16_bf16 v[36:51], v[144:147], v[124:127], v[36:51]\n\t" \
      "ds_read_b64_tr_b16 v[144:145], %[ab] offset:14400\n\t" \
      "ds_read_b64_tr_b16 v[146:147], %[ab] offset:16192\n\t" \
      "v_mov_b64 v[164:165], v[128:129]\n\t" \
      "s_waitcnt vmcnt(2)\n\t" \
      "v_max3_u32 v166, v132, v133, v134\n\t" \
      "v_max_u32 v166, v166, v135\n\t" \
      "s_waitcnt lgkmcnt(8)\n\t" \
      "v_mfma_f32_32x32x16_bf16 v[52:67], v[148:151], v[124:127], v[52:67]\n\t" \
      "ds_read_b64_tr_b16 v[148:149], %[ab] offset:14432\n\t" \
      "ds_read_b64_tr_b16 v[150:151], %[ab] offset:16224\n\t" \
      "v_mov_b32_dpp v128, v130 row_ror:8 row_mask:0xf bank_mask:0xc\n\t" \
      "v_mov_b32_dpp v129, v131 row_ror:8 row_mask:0xf bank_mask:0xc\n\t" \
      "v_cmp_eq_u32 vcc, -1, v166\n\t" \
      "v_mfma_f32_32x32x16_bf16 v[68:83], v[152:155], v[124:127], v[68:83]\n\t" \
      "ds_read_b64_tr_b16 v[152:153], %[ab] offset:14464\n\t" \
      "ds_read_b64_tr_b16 v[154:155], %[ab] offset:16256\n\t" \
      "v_mov_b32_dpp v130, v164 row_ror:8 row_mask:0xf bank_mask:0x3\n\t" \
      "v_mov_b32_dpp v131, v165 row_ror:8 row_mask:0xf bank_mask:0x3\n\t" \
      "s_cbranch_vccnz STUB24_%=\n\t" \
      "BACK24_%=:\n\t" \
      "v_permlane16_swap_b32 v132, v133\n\t" \
      "v_permlane16_swap_b32 v134, v135\n\t" \
      "v_mfma_f32_32x32x16_bf16 v[84:99], v[156:159], v[124:127], v[84:99]\n\t" \
      "ds_read_b64_tr_b16 v[156:157], %[ab] offset:14496\n\t" \
      "ds_read_b64_tr_b16 v[158:159], %[ab] offset:16288\n\t" \
      "v_mov_b32 v167, 163828\n\t" \
      "v_mov_b64 v[164:165], v[132:133]\n\t" \
      "s_waitcnt lgkmcnt(12)\n\t" \
      "v_mfma_f32_32x32x16_bf16 v[100:115], v[160:163], v[124:127], v[100:115]\n\t" \
      "ds_read_b64_tr_b16 v[160:161], %[ab] offset:14528\n\t" \
      "ds_read_b64_tr_b16 v[162:163], %[ab] offset:16320\n\t" \
      "ds_read_b32 v167, v167\n\t" \
      "v_mov_b32_dpp v132, v134 row_ror:8 row_mask:0xf bank_mask:0xc\n\t" \
      "v_mov_b32_dpp v133, v135 row_ror:8 row_mask:0xf bank_mask:0xc\n\t" \
      "s_nop 0\n\t" \
      "v_mov_b32_dpp v134, v164 row_ror:8 row_mask:0xf bank_mask:0x3\n\t" \
      "v_mov_b32_dpp v135, v165 row_ror:8 row_mask:0xf bank_mask:0x3\n\t" \
      "buffer_load_dwordx4 v[124:127], %[tvo], %[srd], s91 offen sc1\n\t" \
      "s_waitcnt lgkmcnt(9)\n\t" \
      "v_mfma_f32_32x32x16_bf16 v[4:19], v[136:139], v[128:131], v[4:19]\n\t" \
      "ds_read_b64_tr_b16 v[136:137], %[ab] offset:21504\n\t" \
      "ds_read_b64_tr_b16 v[138:139], %[ab] offset:23296\n\t" \
      "s_waitcnt vmcnt(2)\n\t" \
      "v_max3_u32 v166, v116, v117, v118\n\t" \
      "v_max_u32 v166, v166, v119\n\t" \
      "v_cmp_eq_u32 vcc, -1, v166\n\t" \
      "v_mfma_f32_32x32x16_bf16 v[20:35], v[140:143], v[128:131], v[20:35]\n\t" \
      "ds_read_b64_tr_b16 v[140:141], %[ab] offset:28672\n\t" \
      "ds_read_b64_tr_b16 v[142:143], %[ab] offset:30464\n\t" \
      "s_cbranch_vccnz STUB25_%=\n\t" \
      "BACK25_%=:\n\t" \
      "v_permlane16_swap_b32 v116, v117\n\t" \
      "v_permlane16_swap_b32 v118, v119\n\t" \
      "s_waitcnt lgkmcnt(4)\n\t" \
      "v_readfirstlane_b32 %[sp], v167\n\t" \
      "s_cmp_lt_u32 %[sp], 4\n\t" \
      "s_cbranch_scc1 STW_%=\n\t" \
      "STB_%=:\n\t" \
      "v_mfma_f32_32x32x16_bf16 v[36:51], v[144:147], v[128:131], v[36:51]\n\t" \
      "ds_read_b64_tr_b16 v[144:145], %[ab] offset:35840\n\t" \
      "ds_read_b64_tr_b16 v[146:147], %[ab] offset:37632\n\t" \
      "v_mov_b64 v[164:165], v[116:117]\n\t" \
      "s_waitcnt vmcnt(1)\n\t" \
      "v_max3_u32 v166, v120, v121, v122\n\t" \
      "v_max_u32 v166, v166, v123\n\t" \
      "v_mfma_f32_32x32x16_bf16 v[52:67], v[148:151], v[128:131], v[52:67]\n\t" \
      "ds_read_b64_tr_b16 v[148:149], %[ab] offset:21536\n\t" \
      "ds_read_b64_tr_b16 v[150:151], %[ab] offset:23328\n\t" \
      "v_mov_b32_dpp v116, v118 row_ror:8 row_mask:0xf bank_mask:0xc\n\t" \
      "v_mov_b32_dpp v117, v119 row_ror:8 row_mask:0xf bank_mask:0xc\n\t" \
      "v_cmp_eq_u32 vcc, -1, v166\n\t" \
      "v_mfma_f32_32x32x16_bf16 v[68:83], v[152:155], v[128:131], v[68:83]\n\t" \
      "ds_read_b64_tr_b16 v[152:153], %[ab] offset:28704\n\t" \
      "ds_read_b64_tr_b16 v[154:155], %[ab] offset:30496\n\t" \
      "v_mov_b32_dpp v118, v164 row_ror:8 row_mask:0xf bank_mask:0x3\n\t" \
      "v_mov_b32_dpp v119, v165 row_ror:8 row_mask:0xf bank_mask:0x3\n\t" \
      "s_cbranch_vccnz STUB26_%=\n\t" \
      "BACK26_%=:\n\t" \
      "v_permlane16_swap_b32 v120, v121\n\t" \
      "v_permlane16_swap_b32 v122, v123\n\t" \
      "v_mfma_f32_32x32x16_bf16 v[84:99], v[156:159], v[128:131], v[84:99]\n\t" \
      "v_mov_b64 v[164:165], v[120:121]\n\t" \
      "v_mfma_f32_32x32x16_bf16 v[100:115], v[160:163], v[128:131], v[100:115]\n\t" \
      "v_mov_b32_dpp v120, v122 row_ror:8 row_mask:0xf bank_mask:0xc\n\t" \
      "v_mov_b32_dpp v121, v123 row_ror:8 row_mask:0xf bank_mask:0xc\n\t" \
      "s_nop 0\n\t" \
      "v_mov_b32_dpp v122, v164 row_ror:8 row_mask:0xf bank_mask:0x3\n\t" \
      "v_mov_b32_dpp v123, v165 row_ror:8 row_mask:0xf bank_mask:0x3\n\t" \
      "s_nop 1\n\t" \
      "s_waitcnt lgkmcnt(8)\n\t" \
      "v_mfma_f32_32x32x16_bf16 v[4:19], v[136:139], v[132:135], v[4:19]\n\t" \
      "ds_read_b64_tr_b16 v[136:137], %[ab] offset:43008\n\t" \
      "ds_read_b64_tr_b16 v[138:139], %[ab] offset:44800\n\t" \
      "s_waitcnt vmcnt(0)\n\t" \
      "v_max3_u32 v166, v124, v125, v126\n\t" \
      "v_max_u32 v166, v166, v127\n\t" \
      "v_cmp_eq_u32 vcc, -1, v166\n\t" \
      "s_waitcnt lgkmcnt(8)\n\t" \
      "v_mfma_f32_32x32x16_bf16 v[4:19], v[140:143], v[116:119], v[4:19]\n\t" \
      "ds_read_b64_tr_b16 v[140:141], %[ab] offset:35872\n\t" \
      "ds_read_b64_tr_b16 v[142:143], %[ab] offset:37664\n\t" \
      "s_cbranch_vccnz STUB27_%=\n\t" \
      "BACK27_%=:\n\t" \
      "v_permlane16_swap_b32 v124, v125\n\t" \
      "v_permlane16_swap_b32 v126, v127\n\t" \
      "s_waitcnt lgkmcnt(8)\n\t" \
      "v_mfma_f32_32x32x16_bf16 v[4:19], v[144:147], v[120:123], v[4:19]\n\t" \
      "ds_read_b64_tr_b16 v[144:145], %[ab] offset:43040\n\t" \
      "ds_read_b64_tr_b16 v[146:147], %[ab] offset:44832\n\t" \
      "v_mov_b64 v[164:165], v[124:125]\n\t" \
      "s_waitcnt lgkmcnt(8)\n\t" \
      "v_mfma_f32_32x32x16_bf16 v[20:35], v[148:151], v[132:135], v[20:35]\n\t" \
      "ds_read_b64_tr_b16 v[148:149], %[ab] offset:21568\n\t" \
      "ds_read_b64_tr_b16 v[150:151], %[ab] offset:23360\n\t" \
      "v_mov_b32_dpp v124, v126 row_ror:8 row_mask:0xf bank_mask:0xc\n\t" \
      "v_mov_b32_dpp v125, v127 row_ror:8 row_mask:0xf bank_mask:0xc\n\t" \
      "s_waitcnt lgkmcnt(8)\n\t" \
      "v_mfma_f32_32x32x16_bf16 v[20:35], v[152:155], v[116:119], v[20:35]\n\t" \
      "ds_read_b64_tr_b16 v[152:153], %[ab] offset:28736\n\t" \
      "ds_read_b64_tr_b16 v[154:155], %[ab] offset:30528\n\t" \
      "s_nop 0\n\t" \
      "v_mov_b32_dpp v126, v164 row_ror:8 row_mask:0xf bank_mask:0x3\n\t" \
      "v_mov_b32_dpp v127, v165 row_ror:8 row_mask:0xf bank_mask:0x3\n\t" \
      "s_nop 1\n\t" \
      "s_waitcnt lgkmcnt(8)\n\t" \
      "v_mfma_f32_32x32x16_bf16 v[4:19], v[136:139], v[124:127], v[4:19]\n\t" \
      "ds_read_b64_tr_b16 v[136:137], %[ab] offset:35904\n\t" \
      "ds_read_b64_tr_b16 v[138:139], %[ab] offset:37696\n\t" \
      "v_mbcnt_lo_u32_b32 v167, -1, 0\n\t" \
      "v_mbcnt_hi_u32_b32 v167, -1, v167\n\t" \
      "v_and_b32 v166, 31, v167\n\t" \
      "v_lshl_add_u32 v166, v166, 3, %[stb]\n\t" \
      "ds_read_b64 v[164:165], v166\n\t" \
      "v_lshrrev_b32 v166, 5, v167\n\t" \
      "v_lshlrev_b32 v166, 4, v166\n\t" \
      "v_add_u32 v166, 150528, v166\n\t" \
      "v_lshlrev_b32 v167, 4, v167\n\t" \
      "ds_read_b128 v[156:159], v166 offset:0\n\t" \
      "ds_read_b128 v[160:163], v166 offset:32\n\t" \
      "s_waitcnt lgkmcnt(11)\n\t" \
      "v_mfma_f32_32x32x16_bf16 v[20:35], v[140:143], v[120:123], v[20:35]\n\t" \
      "ds_read_b64_tr_b16 v[140:141], %[ab] offset:43072\n\t" \
      "ds_read_b64_tr_b16 v[142:143], %[ab] offset:44864\n\t" \
      "s_waitcnt lgkmcnt(11)\n\t" \
      "v_mfma_f32_32x32x16_bf16 v[20:35], v[144:147], v[124:127], v[20:35]\n\t" \
      "ds_read_b64_tr_b16 v[144:145], %[ab] offset:21600\n\t" \
      "ds_read_b64_tr_b16 v[146:147], %[ab] offset:23392\n\t" \
      "s_waitcnt lgkmcnt(4)\n\t" \
      "v_pk_fma_f32 v[4:5], v[164:165], v[156:157], v[4:5] op_sel_hi:[0,1,1]\n\t" \
      "v_pk_fma_f32 v[6:7], v[164:165], v[158:159], v[6:7] op_sel_hi:[0,1,1]\n\t" \
      "v_pk_fma_f32 v[8:9], v[164:165], v[160:161], v[8:9] op_sel_hi:[0,1,1]\n\t" \
      "v_pk_fma_f32 v[10:11], v[164:165], v[162:163], v[10:11] op_sel_hi:[0,1,1]\n\t" \
      "ds_read_b128 v[156:159], v166 offset:64\n\t" \
      "ds_read_b128 v[160:163], v166 offset:96\n\t" \
      "v_max_f32 v4, 0, v4\n\t" \
      "v_max_f32 v5, 0, v5\n\t" \
      "v_mfma_f32_32x32x16_bf16 v[36:51], v[148:151], v[132:135], v[36:51]\n\t" \
      "ds_read_b64_tr_b16 v[148:149], %[ab] offset:28768\n\t" \
      "ds_read_b64_tr_b16 v[150:151], %[ab] offset:30560\n\t" \
      "v_max_f32 v6, 0, v6\n\t" \
      "v_max_f32 v7, 0, v7\n\t" \
      "v_pk_mul_f32 v[8:9], v[8:9], v[164:165] op_sel:[0,1] op_sel_hi:[1,1]\n\t" \
      "v_pk_mul_f32 v[10:11], v[10:11], v[164:165] op_sel:[0,1] op_sel_hi:[1,1]\n\t" \
      "v_pk_mul_f32 v[4:5], v[4:5], v[8:9]\n\t" \
      "v_pk_mul_f32 v[6:7], v[6:7], v[10:11]\n\t" \
      "v_cvt_pk_bf16_f32 v4, v4, v5\n\t" \
      "v_cvt_pk_bf16_f32 v5, v6, v7\n\t" \
      "s_waitcnt lgkmcnt(2)\n\t" \
      "v_mfma_f32_32x32x16_bf16 v[36:51], v[152:155], v[116:119], v[36:51]\n\t" \
      "ds_read_b64_tr_b16 v[152:153], %[ab] offset:35936\n\t" \
      "ds_read_b64_tr_b16 v[154:155], %[ab] offset:37728\n\t" \
      "v_pk_fma_f32 v[12:13], v[164:165], v[156:157], v[12:13] op_sel_hi:[0,1,1]\n\t" \
      "v_pk_fma_f32 v[14:15], v[164:165], v[158:159], v[14:15] op_sel_hi:[0,1,1]\n\t" \
      "v_pk_fma_f32 v[16:17], v[164:165], v[160:161], v[16:17] op_sel_hi:[0,1,1]\n\t" \
      "v_pk_fma_f32 v[18:19], v[164:165], v[162:163], v[18:19] op_sel_hi:[0,1,1]\n\t" \
      "ds_read_b128 v[156:159], v166 offset:128\n\t" \
      "ds_read_b128 v[160:163], v166 offset:160\n\t" \
      "v_max_f32 v12, 0, v12\n\t" \
      "v_max_f32 v13, 0, v13\n\t" \
      "v_max_f32 v14, 0, v14\n\t" \
      "v_mfma_f32_32x32x16_bf16 v[36:51], v[136:139], v[120:123], v[36:51]\n\t" \
      "ds_read_b64_tr_b16 v[136:137], %[ab] offset:43104\n\t" \
      "ds_read_b64_tr_b16 v[138:139], %[ab] offset:44896\n\t" \
      "v_max_f32 v15, 0, v15\n\t" \
      "v_pk_mul_f32 v[16:17], v[16:17], v[164:165] op_sel:[0,1] op_sel_hi:[1,1]\n\t" \
      "v_pk_mul_f32 v[18:19], v[18:19], v[164:165] op_sel:[0,1] op_sel_hi:[1,1]\n\t" \
      "v_pk_mul_f32 v[12:13], v[12:13], v[16:17]\n\t" \
      "v_pk_mul_f32 v[14:15], v[14:15], v[18:19]\n\t" \
      "v_cvt_pk_bf16_f32 v6, v12, v13\n\t" \
      "v_cvt_pk_bf16_f32 v7, v14, v15\n\t" \
      "buffer_store_dwordx4 v[4:7], v167, %[srd], %[hmo] offen offset:0 sc1\n\t" \
      "s_waitcnt lgkmcnt(2)\n\t" \
      "v_mfma_f32_32x32x16_bf16 v[36:51], v[140:143], v[124:127], v[36:51]\n\t" \
      "ds_read_b64_tr_b16 v[140:141], %[ab] offset:21632\n\t" \
      "ds_read_b64_tr_b16 v[142:143], %[ab] offset:23424\n\t" \
      "v_pk_fma_f32 v[20:21], v[164:165], v[156:157], v[20:21] op_sel_hi:[0,1,1]\n\t" \
      "v_pk_fma_f32 v[22:23], v[164:165], v[158:159], v[22:23] op_sel_hi:[0,1,1]\n\t" \
      "v_pk_fma_f32 v[24:25], v[164:165], v[160:161], v[24:25] op_sel_hi:[0,1,1]\n\t" \
      "v_pk_fma_f32 v[26:27], v[164:165], v[162:163], v[26:27] op_sel_hi:[0,1,1]\n\t" \
      "ds_read_b128 v[156:159], v166 offset:192\n\t" \
      "ds_read_b128 v[160:163], v166 offset:224\n\t" \
      "v_max_f32 v20, 0, v20\n\t" \
      "v_max_f32 v21, 0, v21\n\t" \
      "v_max_f32 v22, 0, v22\n\t" \
      "v_mfma_f32_32x32x16_bf16 v[52:67], v[144:147], v[132:135], v[52:67]\n\t" \
      "ds_read_b64_tr_b16 v[144:145], %[ab] offset:28800\n\t" \
      "ds_read_b64_tr_b16 v[146:147], %[ab] offset:30592\n\t" \
      "v_max_f32 v23, 0, v23\n\t" \
      "v_pk_mul_f32 v[24:25], v[24:25], v[164:165] op_sel:[0,1] op_sel_hi:[1,1]\n\t" \
      "v_pk_mul_f32 v[26:27], v[26:27], v[164:165] op_sel:[0,1] op_sel_hi:[1,1]\n\t" \
      "v_pk_mul_f32 v[20:21], v[20:21], v[24:25]\n\t" \
      "v_pk_mul_f32 v[22:23], v[22:23], v[26:27]\n\t" \
      "v_cvt_pk_bf16_f32 v20, v20, v21\n\t" \
      "v_cvt_pk_bf16_f32 v21, v22, v23\n\t" \
      "s_waitcnt lgkmcnt(2)\n\t" \
      "v_pk_fma_f32 v[28:29], v[164:165], v[156:157], v[28:29] op_sel_hi:[0,1,1]\n\t" \
      "v_mfma_f32_32x32x16_bf16 v[52:67], v[148:151], v[116:119], v[52:67]\n\t" \
      "ds_read_b64_tr_b16 v[148:149], %[ab] offset:35968\n\t" \
      "ds_read_b64_tr_b16 v[150:151], %[ab] offset:37760\n\t" \
      "v_pk_fma_f32 v[30:31], v[164:165], v[158:159], v[30:31] op_sel_hi:[0,1,1]\n\t" \
      "v_pk_fma_f32 v[32:33], v[164:165], v[160:161], v[32:33] op_sel_hi:[0,1,1]\n\t" \
      "v_pk_fma_f32 v[34:35], v[164:165], v[162:163], v[34:35] op_sel_hi:[0,1,1]\n\t" \
      "ds_read_b128 v[156:159], v166 offset:256\n\t" \
      "ds_read_b128 v[160:163], v166 offset:288\n\t" \
      "v_max_f32 v28, 0, v28\n\t" \
      "v_max_f32 v29, 0, v29\n\t" \
      "v_max_f32 v30, 0, v30\n\t" \
      "v_max_f32 v31, 0, v31\n\t" \
      "v_mfma_f32_32x32x16_bf16 v[52:67], v[152:155], v[120:123], v[52:67]\n\t" \
      "ds_read_b64_tr_b16 v[152:153], %[ab] offset:43136\n\t" \
      "ds_read_b64_tr_b16 v[154:155], %[ab] offset:44928\n\t" \
      "v_pk_mul_f32 v[32:33], v[32:33], v[164:165] op_sel:[0,1] op_sel_hi:[1,1]\n\t" \
      "v_pk_mul_f32 v[34:35], v[34:35], v[164:165] op_sel:[0,1] op_sel_hi:[1,1]\n\t" \
      "v_pk_mul_f32 v[28:29], v[28:29], v[32:33]\n\t" \
      "v_pk_mul_f32 v[30:31], v[30:31], v[34:35]\n\t" \
      "v_cvt_pk_bf16_f32 v22, v28, v29\n\t" \
      "v_cvt_pk_bf16_f32 v23, v30, v31\n\t" \
      "buffer_store_dwordx4 v[20:23], v167, %[srd], %[hmo] offen offset:1024 sc1\n\t" \
      "s_waitcnt lgkmcnt(2)\n\t" \
      "v_pk_fma_f32 v[36:37], v[164:165], v[156:157], v[36:37] op_sel_hi:[0,1,1]\n\t" \
      "v_mfma_f32_32x32x16_bf16 v[52:67], v[136:139], v[124:127], v[52:67]\n\t" \
      "ds_read_b64_tr_b16 v[136:137], %[ab] offset:21664\n\t" \
      "ds_read_b64_tr_b16 v[138:139], %[ab] offset:23456\n\t" \
      "v_pk_fma_f32 v[38:39], v[164:165], v[158:159], v[38:39] op_sel_hi:[0,1,1]\n\t" \
      "v_pk_fma_f32 v[40:41], v[164:165], v[160:161], v[40:41] op_sel_hi:[0,1,1]\n\t" \
      "v_pk_fma_f32 v[42:43], v[164:165], v[162:163], v[42:43] op_sel_hi:[0,1,1]\n\t" \
      "ds_read_b128 v[156:159], v166 offset:320\n\t" \
      "ds_read_b128 v[160:163], v166 offset:352\n\t" \
      "v_max_f32 v36, 0, v36\n\t" \
      "v_max_f32 v37, 0, v37\n\t" \
      "v_max_f32 v38, 0, v38\n\t" \
      "v_max_f32 v39, 0, v39\n\t" \
      "v_mfma_f32_32x32x16_bf16 v[68:83], v[140:143], v[132:135], v[68:83]\n\t" \
      "ds_read_b64_tr_b16 v[140:141], %[ab] offset:28832\n\t" \
      "ds_read_b64_tr_b16 v[142:143], %[ab] offset:30624\n\t" \
      "v_pk_mul_f32 v[40:41], v[40:41], v[164:165] op_sel:[0,1] op_sel_hi:[1,1]\n\t" \
      "v_pk_mul_f32 v[42:43], v[42:43], v[164:165] op_sel:[0,1] op_sel_hi:[1,1]\n\t" \
      "v_pk_mul_f32 v[36:37], v[36:37], v[40:41]\n\t" \
      "v_pk_mul_f32 v[38:39], v[38:39], v[42:43]\n\t" \
      "v_cvt_pk_bf16_f32 v36, v36, v37\n\t" \
      "v_cvt_pk_bf16_f32 v37, v38, v39\n\t" \
      "s_waitcnt lgkmcnt(2)\n\t" \
      "v_pk_fma_f32 v[44:45], v[164:165], v[156:157], v[44:45] op_sel_hi:[0,1,1]\n\t" \
      "v_pk_fma_f32 v[46:47], v[164:165], v[158:159], v[46:47] op_sel_hi:[0,1,1]\n\t" \
      "v_mfma_f32_32x32x16_bf16 v[68:83], v[144:147], v[116:119], v[68:83]\n\t" \
      "ds_read_b64_tr_b16 v[144:145], %[ab] offset:36000\n\t" \
      "ds_read_b64_tr_b16 v[146:147], %[ab] offset:37792\n\t" \
      "v_pk_fma_f32 v[48:49], v[164:165], v[160:161], v[48:49] op_sel_hi:[0,1,1]\n\t" \
      "v_pk_fma_f32 v[50:51], v[164:165], v[162:163], v[50:51] op_sel_hi:[0,1,1]\n\t" \
      "ds_read_b128 v[156:159], v166 offset:384\n\t" \
      "ds_read_b128 v[160:163], v166 offset:416\n\t" \
      "v_max_f32 v44, 0, v44\n\t" \
      "v_max_f32 v45, 0, v45\n\t" \
      "v_max_f32 v46, 0, v46\n\t" \
      "v_max_f32 v47, 0, v47\n\t" \
      "v_pk_mul_f32 v[48:49], v[48:49], v[164:165] op_sel:[0,1] op_sel_hi:[1,1]\n\t" \
      "v_mfma_f32_32x32x16_bf16 v[68:83], v[148:151], v[120:123], v[68:83]\n\t" \
      "ds_read_b64_tr_b16 v[148:149], %[ab] offset:43168\n\t" \
      "ds_read_b64_tr_b16 v[150:151], %[ab] offset:44960\n\t" \
      "v_pk_mul_f32 v[50:51], v[50:51], v[164:165] op_sel:[0,1] op_sel_hi:[1,1]\n\t" \
      "v_pk_mul_f32 v[44:45], v[44:45], v[48:49]\n\t" \
      "v_pk_mul_f32 v[46:47], v[46:47], v[50:51]\n\t" \
      "v_cvt_pk_bf16_f32 v38, v44, v45\n\t" \
      "v_cvt_pk_bf16_f32 v39, v46, v47\n\t" \
      "buffer_store_dwordx4 v[36:39], v167, %[srd], %[hmo] offen offset:2048 sc1\n\t" \
      "s_waitcnt lgkmcnt(2)\n\t" \
      "v_pk_fma_f32 v[52:53], v[164:165], v[156:157], v[52:53] op_sel_hi:[0,1,1]\n\t" \
      "v_pk_fma_f32 v[54:55], v[164:165], v[158:159], v[54:55] op_sel_hi:[0,1,1]\n\t" \
      "v_mfma_f32_32x32x16_bf16 v[68:83], v[152:155], v[124:127], v[68:83]\n\t" \
      "ds_read_b64_tr_b16 v[152:153], %[ab] offset:21696\n\t" \
      "ds_read_b64_tr_b16 v[154:155], %[ab] offset:23488\n\t" \
      "v_pk_fma_f32 v[56:57], v[164:165], v[160:161], v[56:57] op_sel_hi:[0,1,1]\n\t" \
      "v_pk_fma_f32 v[58:59], v[164:165], v[162:163], v[58:59] op_sel_hi:[0,1,1]\n\t" \
      "ds_read_b128 v[156:159], v166 offset:448\n\t" \
      "ds_read_b128 v[160:163], v166 offset:480\n\t" \
      "v_max_f32 v52, 0, v52\n\t" \
      "v_max_f32 v53, 0, v53\n\t" \
      "v_max_f32 v54, 0, v54\n\t" \
      "v_max_f32 v55, 0, v55\n\t" \
      "v_pk_mul_f32 v[56:57], v[56:57], v[164:165] op_sel:[0,1] op_sel_hi:[1,1]\n\t" \
      "v_mfma_f32_32x32x16_bf16 v[84:99], v[136:139], v[132:135], v[84:99]\n\t" \
      "ds_read_b64_tr_b16 v[136:137], %[ab] offset:28864\n\t" \
      "ds_read_b64_tr_b16 v[138:139], %[ab] offset:30656\n\t" \
      "v_pk_mul_f32 v[58:59], v[58:59], v[164:165] op_sel:[0,1] op_sel_hi:[1,1]\n\t" \
      "v_pk_mul_f32 v[52:53], v[52:53], v[56:57]\n\t" \
      "v_pk_mul_f32 v[54:55], v[54:55], v[58:59]\n\t" \
      "v_cvt_pk_bf16_f32 v52, v52, v53\n\t" \
      "v_cvt_pk_bf16_f32 v53, v54, v55\n\t" \
      "s_waitcnt lgkmcnt(2)\n\t" \
      "v_pk_fma_f32 v[60:61], v[164:165], v[156:157], v[60:61] op_sel_hi:[0,1,1]\n\t" \
      "v_pk_fma_f32 v[62:63], v[164:165], v[158:159], v[62:63] op_sel_hi:[0,1,1]\n\t" \
      "v_pk_fma_f32 v[64:65], v[164:165], v[160:161], v[64:65] op_sel_hi:[0,1,1]\n\t" \
      "v_mfma_f32_32x32x16_bf16 v[84:99], v[140:143], v[116:119], v[84:99]\n\t" \
      "ds_read_b64_tr_b16 v[140:141], %[ab] offset:36032\n\t" \
      "ds_read_b64_tr_b16 v[142:143], %[ab] offset:37824\n\t" \
      "v_pk_fma_f32 v[66:67], v[164:165], v[162:163], v[66:67] op_sel_hi:[0,1,1]\n\t" \
      "ds_read_b128 v[156:159], v166 offset:512\n\t" \
      "ds_read_b128 v[160:163], v166 offset:544\n\t" \
      "v_max_f32 v60, 0, v60\n\t" \
      "v_max_f32 v61, 0, v61\n\t" \
      "v_max_f32 v62, 0, v62\n\t" \
      "v_max_f32 v63, 0, v63\n\t" \
      "v_pk_mul_f32 v[64:65], v[64:65], v[164:165] op_sel:[0,1] op_sel_hi:[1,1]\n\t" \
      "v_pk_mul_f32 v[66:67], v[66:67], v[164:165] op_sel:[0,1] op_sel_hi:[1,1]\n\t" \
      "v_mfma_f32_32x32x16_bf16 v[84:99], v[144:147], v[120:123], v[84:99]\n\t" \
      "ds_read_b64_tr_b16 v[144:145], %[ab] offset:43200\n\t" \
      "ds_read_b64_tr_b16 v[146:147], %[ab] offset:44992\n\t" \
      "v_pk_mul_f32 v[60:61], v[60:61], v[64:65]\n\t" \
      "v_pk_mul_f32 v[62:63], v[62:63], v[66:67]\n\t" \
      "v_cvt_pk_bf16_f32 v54, v60, v61\n\t" \
      "v_cvt_pk_bf16_f32 v55, v62, v63\n\t" \
      "buffer_store_dwordx4 v[52:55], v167, %[srd], %[hmo] offen offset:3072 sc1\n\t" \
      "s_add_u32 %[hmo], %[hmo], 0x1000\n\t" \
      "s_waitcnt lgkmcnt(2)\n\t" \
      "v_pk_fma_f32 v[68:69], v[164:165], v[156:157], v[68:69] op_sel_hi:[0,1,1]\n\t" \
      "v_pk_fma_f32 v[70:71], v[164:165], v[158:159], v[70:71] op_sel_hi:[0,1,1]\n\t" \
      "v_mfma_f32_32x32x16_bf16 v[84:99], v[148:151], v[124:127], v[84:99]\n\t" \
      "v_pk_fma_f32 v[72:73], v[164:165], v[160:161], v[72:73] op_sel_hi:[0,1,1]\n\t" \
      "v_pk_fma_f32 v[74:75], v[164:165], v[162:163], v[74:75] op_sel_hi:[0,1,1]\n\t" \
      "ds_read_b128 v[156:159], v166 offset:576\n\t" \
      "ds_read_b128 v[160:163], v166 offset:608\n\t" \
      "v_max_f32 v68, 0, v68\n\t" \
      "v_max_f32 v69, 0, v69\n\t" \
      "v_max_f32 v70, 0, v70\n\t" \
      "v_max_f32 v71, 0, v71\n\t" \
      "v_pk_mul_f32 v[72:73], v[72:73], v[164:165] op_sel:[0,1] op_sel_hi:[1,1]\n\t" \
      "v_mfma_f32_32x32x16_bf16 v[100:115], v[152:155], v[132:135], v[100:115]\n\t" \
      "v_pk_mul_f32 v[74:75], v[74:75], v[164:165] op_sel:[0,1] op_sel_hi:[1,1]\n\t" \
      "v_pk_mul_f32 v[68:69], v[68:69], v[72:73]\n\t" \
      "v_pk_mul_f32 v[70:71], v[70:71], v[74:75]\n\t" \
      "v_cvt_pk_bf16_f32 v68, v68, v69\n\t" \
      "v_cvt_pk_bf16_f32 v69, v70, v71\n\t" \
      "s_waitcnt lgkmcnt(0)\n\t" \
      "v_pk_fma_f32 v[76:77], v[164:165], v[156:157], v[76:77] op_sel_hi:[0,1,1]\n\t" \
      "v_pk_fma_f32 v[78:79], v[164:165], v[158:159], v[78:79] op_sel_hi:[0,1,1]\n\t" \
      "v_pk_fma_f32 v[80:81], v[164:165], v[160:161], v[80:81] op_sel_hi:[0,1,1]\n\t" \
      "v_mfma_f32_32x32x16_bf16 v[100:115], v[136:139], v[116:119], v[100:115]\n\t" \
      "v_pk_fma_f32 v[82:83], v[164:165], v[162:163], v[82:83] op_sel_hi:[0,1,1]\n\t" \
      "ds_read_b128 v[156:159], v166 offset:640\n\t" \
      "ds_read_b128 v[160:163], v166 offset:672\n\t" \
      "v_max_f32 v76, 0, v76\n\t" \
      "v_max_f32 v77, 0, v77\n\t" \
      "v_max_f32 v78, 0, v78\n\t" \
      "v_max_f32 v79, 0, v79\n\t" \
      "v_pk_mul_f32 v[80:81], v[80:81], v[164:165] op_sel:[0,1] op_sel_hi:[1,1]\n\t" \
      "v_pk_mul_f32 v[82:83], v[82:83], v[164:165] op_sel:[0,1] op_sel_hi:[1,1]\n\t" \
      "v_mfma_f32_32x32x16_bf16 v[100:115], v[140:143], v[120:123], v[100:115]\n\t" \
      "v_pk_mul_f32 v[76:77], v[76:77], v[80:81]\n\t" \
      "v_pk_mul_f32 v[78:79], v[78:79], v[82:83]\n\t" \
      "v_cvt_pk_bf16_f32 v70, v76, v77\n\t" \
      "v_cvt_pk_bf16_f32 v71, v78, v79\n\t" \
      "buffer_store_dwordx4 v[68:71], v167, %[srd], %[hmo] offen offset:0 sc1\n\t" \
      "s_waitcnt lgkmcnt(0)\n\t" \
      "v_pk_fma_f32 v[84:85], v[164:165], v[156:157], v[84:85] op_sel_hi:[0,1,1]\n\t" \
      "v_pk_fma_f32 v[86:87], v[164:165], v[158:159], v[86:87] op_sel_hi:[0,1,1]\n\t" \
      "v_pk_fma_f32 v[88:89], v[164:165], v[160:161], v[88:89] op_sel_hi:[0,1,1]\n\t" \
      "v_mfma_f32_32x32x16_bf16 v[100:115], v[144:147], v[124:127], v[100:115]\n\t" \
      "v_pk_fma_f32 v[90:91], v[164:165], v[162:163], v[90:91] op_sel_hi:[0,1,1]\n\t" \
      "ds_read_b128 v[156:159], v166 offset:704\n\t" \
      "ds_read_b128 v[160:163], v166 offset:736\n\t" \
      "v_max_f32 v84, 0, v84\n\t" \
      "v_max_f32 v85, 0, v85\n\t" \
      "v_max_f32 v86, 0, v86\n\t" \
      "v_max_f32 v87, 0, v87\n\t" \
      "v_pk_mul_f32 v[88:89], v[88:89], v[164:165] op_sel:[0,1] op_sel_hi:[1,1]\n\t" \
      "v_pk_mul_f32 v[90:91], v[90:91], v[164:165] op_sel:[0,1] op_sel_hi:[1,1]\n\t" \
      "s_nop 15\n\t" \
      "v_pk_mul_f32 v[84:85], v[84:85], v[88:89]\n\t" \
      "v_pk_mul_f32 v[86:87], v[86:87], v[90:91]\n\t" \
      "v_cvt_pk_bf16_f32 v84, v84, v85\n\t" \
      "v_cvt_pk_bf16_f32 v85, v86, v87\n\t" \
      "s_waitcnt lgkmcnt(0)\n\t" \
      "v_pk_fma_f32 v[92:93], v[164:165], v[156:157], v[92:93] op_sel_hi:[0,1,1]\n\t" \
      "v_pk_fma_f32 v[94:95], v[164:165], v[158:159], v[94:95] op_sel_hi:[0,1,1]\n\t" \
      "v_pk_fma_f32 v[96:97], v[164:165], v[160:161], v[96:97] op_sel_hi:[0,1,1]\n\t" \
      "v_pk_fma_f32 v[98:99], v[164:165], v[162:163], v[98:99] op_sel_hi:[0,1,1]\n\t" \
      "ds_read_b128 v[156:159], v166 offset:768\n\t" \
      "ds_read_b128 v[160:163], v166 offset:800\n\t" \
      "v_max_f32 v92, 0, v92\n\t" \
      "v_max_f32 v93, 0, v93\n\t" \
      "v_max_f32 v94, 0, v94\n\t" \
      "v_max_f32 v95, 0, v95\n\t" \
      "v_pk_mul_f32 v[96:97], v[96:97], v[164:165] op_sel:[0,1] op_sel_hi:[1,1]\n\t" \
      "v_pk_mul_f32 v[98:99], v[98:99], v[164:165] op_sel:[0,1] op_sel_hi:[1,1]\n\t" \
      "v_pk_mul_f32 v[92:93], v[92:93], v[96:97]\n\t" \
      "v_pk_mul_f32 v[94:95], v[94:95], v[98:99]\n\t" \
      "v_cvt_pk_bf16_f32 v86, v92, v93\n\t" \
      "v_cvt_pk_bf16_f32 v87, v94, v95\n\t" \
      "buffer_store_dwordx4 v[84:87], v167, %[srd], %[hmo] offen offset:1024 sc1\n\t" \
      "s_waitcnt lgkmcnt(0)\n\t" \
      "v_pk_fma_f32 v[100:101], v[164:165], v[156:157], v[100:101] op_sel_hi:[0,1,1]\n\t" \
      "v_pk_fma_f32 v[102:103], v[164:165], v[158:159], v[102:103] op_sel_hi:[0,1,1]\n\t" \
      "v_pk_fma_f32 v[104:105], v[164:165], v[160:161], v[104:105] op_sel_hi:[0,1,1]\n\t" \
      "v_pk_fma_f32 v[106:107], v[164:165], v[162:163], v[106:107] op_sel_hi:[0,1,1]\n\t" \
      "ds_read_b128 v[156:159], v166 offset:832\n\t" \
      "ds_read_b128 v[160:163], v166 offset:864\n\t" \
      "v_max_f32 v100, 0, v100\n\t" \
      "v_max_f32 v101, 0, v101\n\t" \
      "v_max_f32 v102, 0, v102\n\t" \
      "v_max_f32 v103, 0, v103\n\t" \
      "v_pk_mul_f32 v[104:105], v[104:105], v[164:165] op_sel:[0,1] op_sel_hi:[1,1]\n\t" \
      "v_pk_mul_f32 v[106:107], v[106:107], v[164:165] op_sel:[0,1] op_sel_hi:[1,1]\n\t" \
      "v_pk_mul_f32 v[100:101], v[100:101], v[104:105]\n\t" \
      "v_pk_mul_f32 v[102:103], v[102:103], v[106:107]\n\t" \
      "v_cvt_pk_bf16_f32 v100, v100, v101\n\t" \
      "v_cvt_pk_bf16_f32 v101, v102, v103\n\t" \
      "s_waitcnt lgkmcnt(0)\n\t" \
      "v_pk_fma_f32 v[108:109], v[164:165], v[156:157], v[108:109] op_sel_hi:[0,1,1]\n\t" \
      "v_pk_fma_f32 v[110:111], v[164:165], v[158:159], v[110:111] op_sel_hi:[0,1,1]\n\t" \
      "v_pk_fma_f32 v[112:113], v[164:165], v[160:161], v[112:113] op_sel_hi:[0,1,1]\n\t" \
      "v_pk_fma_f32 v[114:115], v[164:165], v[162:163], v[114:115] op_sel_hi:[0,1,1]\n\t" \
      "v_max_f32 v108, 0, v108\n\t" \
      "v_max_f32 v109, 0, v109\n\t" \
      "v_max_f32 v110, 0, v110\n\t" \
      "v_max_f32 v111, 0, v111\n\t" \
      "v_pk_mul_f32 v[112:113], v[112:113], v[164:165] op_sel:[0,1] op_sel_hi:[1,1]\n\t" \
      "v_pk_mul_f32 v[114:115], v[114:115], v[164:165] op_sel:[0,1] op_sel_hi:[1,1]\n\t" \
      "v_pk_mul_f32 v[108:109], v[108:109], v[112:113]\n\t" \
      "v_pk_mul_f32 v[110:111], v[110:111], v[114:115]\n\t" \
      "v_cvt_pk_bf16_f32 v102, v108, v109\n\t" \
      "v_cvt_pk_bf16_f32 v103, v110, v111\n\t" \
      "buffer_store_dwordx4 v[100:103], v167, %[srd], %[hmo] offen offset:2048 sc1\n\t" \
      "s_branch END_%=\n\t" \
      "STUB0_%=:\n\t" \
      "s_mov_b32 %[sp], 0\n\t" \
      "LOOP0_%=:\n\t" \
      "s_sleep 2\n\t" \
      "buffer_load_dwordx4 v[116:119], %[tvo], %[srd], s64 offen sc1\n\t" \
      "buffer_load_dwordx4 v[120:123], %[tvo], %[srd], s65 offen sc1\n\t" \
      "buffer_load_dwordx4 v[124:127], %[tvo], %[srd], s66 offen sc1\n\t" \
      "buffer_load_dwordx4 v[128:131], %[tvo], %[srd], s67 offen sc1\n\t" \
      "buffer_load_dwordx4 v[132:135], %[tvo], %[srd], s68 offen sc1\n\t" \
      "s_add_u32 %[sp], %[sp], 1\n\t" \
      "s_waitcnt vmcnt(0)\n\t" \
      "v_max3_u32 v166, v116, v117, v118\n\t" \
      "v_max_u32 v166, v166, v119\n\t" \
      "v_cmp_eq_u32 vcc, -1, v166\n\t" \
      "s_cmp_ge_u32 %[sp], 0x4000\n\t" \
      "s_cbranch_scc1 BACK0_%=\n\t" \
      "s_cbranch_vccnz LOOP0_%=\n\t" \
      "s_branch BACK0_%=\n\t" \
      "STUB1_%=:\n\t" \
      "s_mov_b32 %[sp], 0\n\t" \
      "LOOP1_%=:\n\t" \
      "s_sleep 2\n\t" \
      "buffer_load_dwordx4 v[120:123], %[tvo], %[srd], s65 offen sc1\n\t" \
      "buffer_load_dwordx4 v[124:127], %[tvo], %[srd], s66 offen sc1\n\t" \
      "buffer_load_dwordx4 v[128:131], %[tvo], %[srd], s67 offen sc1\n\t" \
      "buffer_load_dwordx4 v[132:135], %[tvo], %[srd], s68 offen sc1\n\t" \
      "s_add_u32 %[sp], %[sp], 1\n\t" \
      "s_waitcnt vmcnt(0)\n\t" \
      "v_max3_u32 v166, v120, v121, v122\n\t" \
      "v_max_u32 v166, v166, v123\n\t" \
      "v_cmp_eq_u32 vcc, -1, v166\n\t" \
      "s_cmp_ge_u32 %[sp], 0x4000\n\t" \
      "s_cbranch_scc1 BACK1_%=\n\t" \
      "s_cbranch_vccnz LOOP1_%=\n\t" \
      "s_branch BACK1_%=\n\t" \
      "STUB2_%=:\n\t" \
      "s_mov_b32 %[sp], 0\n\t" \
      "LOOP2_%=:\n\t" \
      "s_sleep 2\n\t" \
      "buffer_load_dwordx4 v[124:127], %[tvo], %[srd], s66 offen sc1\n\t" \
      "buffer_load_dwordx4 v[128:131], %[tvo], %[srd], s67 offen sc1\n\t" \
      "buffer_load_dwordx4 v[132:135], %[tvo], %[srd], s68 offen sc1\n\t" \
      "buffer_load_dwordx4 v[116:119], %[tvo], %[srd], s69 offen sc1\n\t" \
      "s_add_u32 %[sp], %[sp], 1\n\t" \
      "s_waitcnt vmcnt(0)\n\t" \
      "v_max3_u32 v166, v124, v125, v126\n\t" \
      "v_max_u32 v166, v166, v127\n\t" \
      "v_cmp_eq_u32 vcc, -1, v166\n\t" \
      "s_cmp_ge_u32 %[sp], 0x4000\n\t" \
      "s_cbranch_scc1 BACK2_%=\n\t" \
      "s_cbranch_vccnz LOOP2_%=\n\t" \
      "s_branch BACK2_%=\n\t" \
      "STUB3_%=:\n\t" \
      "s_mov_b32 %[sp], 0\n\t" \
      "LOOP3_%=:\n\t" \
      "s_sleep 2\n\t" \
      "buffer_load_dwordx4 v[128:131], %[tvo], %[srd], s67 offen sc1\n\t" \
      "buffer_load_dwordx4 v[132:135], %[tvo], %[srd], s68 offen sc1\n\t" \
      "buffer_load_dwordx4 v[116:119], %[tvo], %[srd], s69 offen sc1\n\t" \
      "buffer_load_dwordx4 v[120:123], %[tvo], %[srd], s70 offen sc1\n\t" \
      "s_add_u32 %[sp], %[sp], 1\n\t" \
      "s_waitcnt vmcnt(0)\n\t" \
      "v_max3_u32 v166, v128, v129, v130\n\t" \
      "v_max_u32 v166, v166, v131\n\t" \
      "v_cmp_eq_u32 vcc, -1, v166\n\t" \
      "s_cmp_ge_u32 %[sp], 0x4000\n\t" \
      "s_cbranch_scc1 BACK3_%=\n\t" \
      "s_cbranch_vccnz LOOP3_%=\n\t" \
      "s_branch BACK3_%=\n\t" \
      "STUB4_%=:\n\t" \
      "s_mov_b32 %[sp], 0\n\t" \
      "LOOP4_%=:\n\t" \
      "s_sleep 2\n\t" \
      "buffer_load_dwordx4 v[132:135], %[tvo], %[srd], s68 offen sc1\n\t" \
      "buffer_load_dwordx4 v[116:119], %[tvo], %[srd], s69 offen sc1\n\t" \
      "buffer_load_dwordx4 v[120:123], %[tvo], %[srd], s70 offen sc1\n\t" \
      "buffer_load_dwordx4 v[124:127], %[tvo], %[srd], s71 offen sc1\n\t" \
      "s_add_u32 %[sp], %[sp], 1\n\t" \
      "s_waitcnt vmcnt(0)\n\t" \
      "v_max3_u32 v166, v132, v133, v134\n\t" \
      "v_max_u32 v166, v166, v135\n\t" \
      "v_cmp_eq_u32 vcc, -1, v166\n\t" \
      "s_cmp_ge_u32 %[sp], 0x4000\n\t" \
      "s_cbranch_scc1 BACK4_%=\n\t" \
      "s_cbranch_vccnz LOOP4_%=\n\t" \
      "s_branch BACK4_%=\n\t" \
      "STUB5_%=:\n\t" \
      "s_mov_b32 %[sp], 0\n\t" \
      "LOOP5_%=:\n\t" \
      "s_sleep 2\n\t" \
      "buffer_load_dwordx4 v[116:119], %[tvo], %[srd], s69 offen sc1\n\t" \
      "buffer_load_dwordx4 v[120:123], %[tvo], %[srd], s70 offen sc1\n\t" \
      "buffer_load_dwordx4 v[124:127], %[tvo], %[srd], s71 offen sc1\n\t" \
      "buffer_load_dwordx4 v[128:131], %[tvo], %[srd], s72 offen sc1\n\t" \
      "s_add_u32 %[sp], %[sp], 1\n\t" \
      "s_waitcnt vmcnt(0)\n\t" \
      "v_max3_u32 v166, v116, v117, v118\n\t" \
      "v_max_u32 v166, v166, v119\n\t" \
      "v_cmp_eq_u32 vcc, -1, v166\n\t" \
      "s_cmp_ge_u32 %[sp], 0x4000\n\t" \
      "s_cbranch_scc1 BACK5_%=\n\t" \
      "s_cbranch_vccnz LOOP5_%=\n\t" \
      "s_branch BACK5_%=\n\t" \
      "STUB6_%=:\n\t" \
      "s_mov_b32 %[sp], 0\n\t" \
      "LOOP6_%=:\n\t" \
      "s_sleep 2\n\t" \
      "buffer_load_dwordx4 v[120:123], %[tvo], %[srd], s70 offen sc1\n\t" \
      "buffer_load_dwordx4 v[124:127], %[tvo], %[srd], s71 offen sc1\n\t" \
      "buffer_load_dwordx4 v[128:131], %[tvo], %[srd], s72 offen sc1\n\t" \
      "buffer_load_dwordx4 v[132:135], %[tvo], %[srd], s73 offen sc1\n\t" \
      "s_add_u32 %[sp], %[sp], 1\n\t" \
      "s_waitcnt vmcnt(0)\n\t" \
      "v_max3_u32 v166, v120, v121, v122\n\t" \
      "v_max_u32 v166, v166, v123\n\t" \
      "v_cmp_eq_u32 vcc, -1, v166\n\t" \
      "s_cmp_ge_u32 %[sp], 0x4000\n\t" \
      "s_cbranch_scc1 BACK6_%=\n\t" \
      "s_cbranch_vccnz LOOP6_%=\n\t" \
      "s_branch BACK6_%=\n\t" \
      "STUB7_%=:\n\t" \
      "s_mov_b32 %[sp], 0\n\t" \
      "LOOP7_%=:\n\t" \
      "s_sleep 2\n\t" \
      "buffer_load_dwordx4 v[124:127], %[tvo], %[srd], s71 offen sc1\n\t" \
      "buffer_load_dwordx4 v[128:131], %[tvo], %[srd], s72 offen sc1\n\t" \
      "buffer_load_dwordx4 v[132:135], %[tvo], %[srd], s73 offen sc1\n\t" \
      "buffer_load_dwordx4 v[116:119], %[tvo], %[srd], s74 offen sc1\n\t" \
      "s_add_u32 %[sp], %[sp], 1\n\t" \
      "s_waitcnt vmcnt(0)\n\t" \
      "v_max3_u32 v166, v124, v125, v126\n\t" \
      "v_max_u32 v166, v166, v127\n\t" \
      "v_cmp_eq_u32 vcc, -1, v166\n\t" \
      "s_cmp_ge_u32 %[sp], 0x4000\n\t" \
      "s_cbranch_scc1 BACK7_%=\n\t" \
      "s_cbranch_vccnz LOOP7_%=\n\t" \
      "s_branch BACK7_%=\n\t" \
      "STUB8_%=:\n\t" \
      "s_mov_b32 %[sp], 0\n\t" \
      "LOOP8_%=:\n\t" \
      "s_sleep 2\n\t" \
      "buffer_load_dwordx4 v[128:131], %[tvo], %[srd], s72 offen sc1\n\t" \
      "buffer_load_dwordx4 v[132:135], %[tvo], %[srd], s73 offen sc1\n\t" \
      "buffer_load_dwordx4 v[116:119], %[tvo], %[srd], s74 offen sc1\n\t" \
      "buffer_load_dwordx4 v[120:123], %[tvo], %[srd], s75 offen sc1\n\t" \
      "s_add_u32 %[sp], %[sp], 1\n\t" \
      "s_waitcnt vmcnt(0)\n\t" \
      "v_max3_u32 v166, v128, v129, v130\n\t" \
      "v_max_u32 v166, v166, v131\n\t" \
      "v_cmp_eq_u32 vcc, -1, v166\n\t" \
      "s_cmp_ge_u32 %[sp], 0x4000\n\t" \
      "s_cbranch_scc1 BACK8_%=\n\t" \
      "s_cbranch_vccnz LOOP8_%=\n\t" \
      "s_branch BACK8_%=\n\t" \
      "STUB9_%=:\n\t" \
      "s_mov_b32 %[sp], 0\n\t" \
      "LOOP9_%=:\n\t" \
      "s_sleep 2\n\t" \
      "buffer_load_dwordx4 v[132:135], %[tvo], %[srd], s73 offen sc1\n\t" \
      "buffer_load_dwordx4 v[116:119], %[tvo], %[srd], s74 offen sc1\n\t" \
      "buffer_load_dwordx4 v[120:123], %[tvo], %[srd], s75 offen sc1\n\t" \
      "buffer_load_dwordx4 v[124:127], %[tvo], %[srd], s76 offen sc1\n\t" \
      "s_add_u32 %[sp], %[sp], 1\n\t" \
      "s_waitcnt vmcnt(0)\n\t" \
      "v_max3_u32 v166, v132, v133, v134\n\t" \
      "v_max_u32 v166, v166, v135\n\t" \
      "v_cmp_eq_u32 vcc, -1, v166\n\t" \
      "s_cmp_ge_u32 %[sp], 0x4000\n\t" \
      "s_cbranch_scc1 BACK9_%=\n\t" \
      "s_cbranch_vccnz LOOP9_%=\n\t" \
      "s_branch BACK9_%=\n\t" \
      "STUB10_%=:\n\t" \
      "s_mov_b32 %[sp], 0\n\t" \
      "LOOP10_%=:\n\t" \
      "s_sleep 2\n\t" \
      "buffer_load_dwordx4 v[116:119], %[tvo], %[srd], s74 offen sc1\n\t" \
      "buffer_load_dwordx4 v[120:123], %[tvo], %[srd], s75 offen sc1\n\t" \
      "buffer_load_dwordx4 v[124:127], %[tvo], %[srd], s76 offen sc1\n\t" \
      "buffer_load_dwordx4 v[128:131], %[tvo], %[srd], s77 offen sc1\n\t" \
      "s_add_u32 %[sp], %[sp], 1\n\t" \
      "s_waitcnt vmcnt(0)\n\t" \
      "v_max3_u32 v166, v116, v117, v118\n\t" \
      "v_max_u32 v166, v166, v119\n\t" \
      "v_cmp_eq_u32 vcc, -1, v166\n\t" \
      "s_cmp_ge_u32 %[sp], 0x4000\n\t" \
      "s_cbranch_scc1 BACK10_%=\n\t" \
      "s_cbranch_vccnz LOOP10_%=\n\t" \
      "s_branch BACK10_%=\n\t" \
      "STUB11_%=:\n\t" \
      "s_mov_b32 %[sp], 0\n\t" \
      "LOOP11_%=:\n\t" \
      "s_sleep 2\n\t" \
      "buffer_load_dwordx4 v[120:123], %[tvo], %[srd], s75 offen sc1\n\t" \
      "buffer_load_dwordx4 v[124:127], %[tvo], %[srd], s76 offen sc1\n\t" \
      "buffer_load_dwordx4 v[128:131], %[tvo], %[srd], s77 offen sc1\n\t" \
      "buffer_load_dwordx4 v[132:135], %[tvo], %[srd], s78 offen sc1\n\t" \
      "s_add_u32 %[sp], %[sp], 1\n\t" \
      "s_waitcnt vmcnt(0)\n\t" \
      "v_max3_u32 v166, v120, v121, v122\n\t" \
      "v_max_u32 v166, v166, v123\n\t" \
      "v_cmp_eq_u32 vcc, -1, v166\n\t" \
      "s_cmp_ge_u32 %[sp], 0x4000\n\t" \
      "s_cbranch_scc1 BACK11_%=\n\t" \
      "s_cbranch_vccnz LOOP11_%=\n\t" \
      "s_branch BACK11_%=\n\t" \
      "STUB12_%=:\n\t" \
      "s_mov_b32 %[sp], 0\n\t" \
      "LOOP12_%=:\n\t" \
      "s_sleep 2\n\t" \
      "buffer_load_dwordx4 v[124:127], %[tvo], %[srd], s76 offen sc1\n\t" \
      "buffer_load_dwordx4 v[128:131], %[tvo], %[srd], s77 offen sc1\n\t" \
      "buffer_load_dwordx4 v[132:135], %[tvo], %[srd], s78 offen sc1\n\t" \
      "buffer_load_dwordx4 v[116:119], %[tvo], %[srd], s79 offen sc1\n\t" \
      "s_add_u32 %[sp], %[sp], 1\n\t" \
      "s_waitcnt vmcnt(0)\n\t" \
      "v_max3_u32 v166, v124, v125, v126\n\t" \
      "v_max_u32 v166, v166, v127\n\t" \
      "v_cmp_eq_u32 vcc, -1, v166\n\t" \
      "s_cmp_ge_u32 %[sp], 0x4000\n\t" \
      "s_cbranch_scc1 BACK12_%=\n\t" \
      "s_cbranch_vccnz LOOP12_%=\n\t" \
      "s_branch BACK12_%=\n\t" \
      "STUB13_%=:\n\t" \
      "s_mov_b32 %[sp], 0\n\t" \
      "LOOP13_%=:\n\t" \
      "s_sleep 2\n\t" \
      "buffer_load_dwordx4 v[128:131], %[tvo], %[srd], s77 offen sc1\n\t" \
      "buffer_load_dwordx4 v[132:135], %[tvo], %[srd], s78 offen sc1\n\t" \
      "buffer_load_dwordx4 v[116:119], %[tvo], %[srd], s79 offen sc1\n\t" \
      "buffer_load_dwordx4 v[120:123], %[tvo], %[srd], s80 offen sc1\n\t" \
      "s_add_u32 %[sp], %[sp], 1\n\t" \
      "s_waitcnt vmcnt(0)\n\t" \
      "v_max3_u32 v166, v128, v129, v130\n\t" \
      "v_max_u32 v166, v166, v131\n\t" \
      "v_cmp_eq_u32 vcc, -1, v166\n\t" \
      "s_cmp_ge_u32 %[sp], 0x4000\n\t" \
      "s_cbranch_scc1 BACK13_%=\n\t" \
      "s_cbranch_vccnz LOOP13_%=\n\t" \
      "s_branch BACK13_%=\n\t" \
      "STUB14_%=:\n\t" \
      "s_mov_b32 %[sp], 0\n\t" \
      "LOOP14_%=:\n\t" \
      "s_sleep 2\n\t" \
      "buffer_load_dwordx4 v[132:135], %[tvo], %[srd], s78 offen sc1\n\t" \
      "buffer_load_dwordx4 v[116:119], %[tvo], %[srd], s79 offen sc1\n\t" \
      "buffer_load_dwordx4 v[120:123], %[tvo], %[srd], s80 offen sc1\n\t" \
      "buffer_load_dwordx4 v[124:127], %[tvo], %[srd], s81 offen sc1\n\t" \
      "s_add_u32 %[sp], %[sp], 1\n\t" \
      "s_waitcnt vmcnt(0)\n\t" \
      "v_max3_u32 v166, v132, v133, v134\n\t" \
      "v_max_u32 v166, v166, v135\n\t" \
      "v_cmp_eq_u32 vcc, -1, v166\n\t" \
      "s_cmp_ge_u32 %[sp], 0x4000\n\t" \
      "s_cbranch_scc1 BACK14_%=\n\t" \
      "s_cbranch_vccnz LOOP14_%=\n\t" \
      "s_branch BACK14_%=\n\t" \
      "STUB15_%=:\n\t" \
      "s_mov_b32 %[sp], 0\n\t" \
      "LOOP15_%=:\n\t" \
      "s_sleep 2\n\t" \
      "buffer_load_dwordx4 v[116:119], %[tvo], %[srd], s79 offen sc1\n\t" \
      "buffer_load_dwordx4 v[120:123], %[tvo], %[srd], s80 offen sc1\n\t" \
      "buffer_load_dwordx4 v[124:127], %[tvo], %[srd], s81 offen sc1\n\t" \
      "buffer_load_dwordx4 v[128:131], %[tvo], %[srd], s82 offen sc1\n\t" \
      "s_add_u32 %[sp], %[sp], 1\n\t" \
      "s_waitcnt vmcnt(0)\n\t" \
      "v_max3_u32 v166, v116, v117, v118\n\t" \
      "v_max_u32 v166, v166, v119\n\t" \
      "v_cmp_eq_u32 vcc, -1, v166\n\t" \
      "s_cmp_ge_u32 %[sp], 0x4000\n\t" \
      "s_cbranch_scc1 BACK15_%=\n\t" \
      "s_cbranch_vccnz LOOP15_%=\n\t" \
      "s_branch BACK15_%=\n\t" \
      "STUB16_%=:\n\t" \
      "s_mov_b32 %[sp], 0\n\t" \
      "LOOP16_%=:\n\t" \
      "s_sleep 2\n\t" \
      "buffer_load_dwordx4 v[120:123], %[tvo], %[srd], s80 offen sc1\n\t" \
      "buffer_load_dwordx4 v[124:127], %[tvo], %[srd], s81 offen sc1\n\t" \
      "buffer_load_dwordx4 v[128:131], %[tvo], %[srd], s82 offen sc1\n\t" \
      "buffer_load_dwordx4 v[132:135], %[tvo], %[srd], s83 offen sc1\n\t" \
      "s_add_u32 %[sp], %[sp], 1\n\t" \
      "s_waitcnt vmcnt(0)\n\t" \
      "v_max3_u32 v166, v120, v121, v122\n\t" \
      "v_max_u32 v166, v166, v123\n\t" \
      "v_cmp_eq_u32 vcc, -1, v166\n\t" \
      "s_cmp_ge_u32 %[sp], 0x4000\n\t" \
      "s_cbranch_scc1 BACK16_%=\n\t" \
      "s_cbranch_vccnz LOOP16_%=\n\t" \
      "s_branch BACK16_%=\n\t" \
      "STUB17_%=:\n\t" \
      "s_mov_b32 %[sp], 0\n\t" \
      "LOOP17_%=:\n\t" \
      "s_sleep 2\n\t" \
      "buffer_load_dwordx4 v[124:127], %[tvo], %[srd], s81 offen sc1\n\t" \
      "buffer_load_dwordx4 v[128:131], %[tvo], %[srd], s82 offen sc1\n\t" \
      "buffer_load_dwordx4 v[132:135], %[tvo], %[srd], s83 offen sc1\n\t" \
      "buffer_load_dwordx4 v[116:119], %[tvo], %[srd], s84 offen sc1\n\t" \
      "s_add_u32 %[sp], %[sp], 1\n\t" \
      "s_waitcnt vmcnt(0)\n\t" \
      "v_max3_u32 v166, v124, v125, v126\n\t" \
      "v_max_u32 v166, v166, v127\n\t" \
      "v_cmp_eq_u32 vcc, -1, v166\n\t" \
      "s_cmp_ge_u32 %[sp], 0x4000\n\t" \
      "s_cbranch_scc1 BACK17_%=\n\t" \
      "s_cbranch_vccnz LOOP17_%=\n\t" \
      "s_branch BACK17_%=\n\t" \
      "STUB18_%=:\n\t" \
      "s_mov_b32 %[sp], 0\n\t" \
      "LOOP18_%=:\n\t" \
      "s_sleep 2\n\t" \
      "buffer_load_dwordx4 v[128:131], %[tvo], %[srd], s82 offen sc1\n\t" \
      "buffer_load_dwordx4 v[132:135], %[tvo], %[srd], s83 offen sc1\n\t" \
      "buffer_load_dwordx4 v[116:119], %[tvo], %[srd], s84 offen sc1\n\t" \
      "buffer_load_dwordx4 v[120:123], %[tvo], %[srd], s85 offen sc1\n\t" \
      "s_add_u32 %[sp], %[sp], 1\n\t" \
      "s_waitcnt vmcnt(0)\n\t" \
      "v_max3_u32 v166, v128, v129, v130\n\t" \
      "v_max_u32 v166, v166, v131\n\t" \
      "v_cmp_eq_u32 vcc, -1, v166\n\t" \
      "s_cmp_ge_u32 %[sp], 0x4000\n\t" \
      "s_cbranch_scc1 BACK18_%=\n\t" \
      "s_cbranch_vccnz LOOP18_%=\n\t" \
      "s_branch BACK18_%=\n\t" \
      "STUB19_%=:\n\t" \
      "s_mov_b32 %[sp], 0\n\t" \
      "LOOP19_%=:\n\t" \
      "s_sleep 2\n\t" \
      "buffer_load_dwordx4 v[132:135], %[tvo], %[srd], s83 offen sc1\n\t" \
      "buffer_load_dwordx4 v[116:119], %[tvo], %[srd], s84 offen sc1\n\t" \
      "buffer_load_dwordx4 v[120:123], %[tvo], %[srd], s85 offen sc1\n\t" \
      "buffer_load_dwordx4 v[124:127], %[tvo], %[srd], s86 offen sc1\n\t" \
      "s_add_u32 %[sp], %[sp], 1\n\t" \
      "s_waitcnt vmcnt(0)\n\t" \
      "v_max3_u32 v166, v132, v133, v134\n\t" \
      "v_max_u32 v166, v166, v135\n\t" \
      "v_cmp_eq_u32 vcc, -1, v166\n\t" \
      "s_cmp_ge_u32 %[sp], 0x4000\n\t" \
      "s_cbranch_scc1 BACK19_%=\n\t" \
      "s_cbranch_vccnz LOOP19_%=\n\t" \
      "s_branch BACK19_%=\n\t" \
      "STUB20_%=:\n\t" \
      "s_mov_b32 %[sp], 0\n\t" \
      "LOOP20_%=:\n\t" \
      "s_sleep 2\n\t" \
      "buffer_load_dwordx4 v[116:119], %[tvo], %[srd], s84 offen sc1\n\t" \
      "buffer_load_dwordx4 v[120:123], %[tvo], %[srd], s85 offen sc1\n\t" \
      "buffer_load_dwordx4 v[124:127], %[tvo], %[srd], s86 offen sc1\n\t" \
      "buffer_load_dwordx4 v[128:131], %[tvo], %[srd], s87 offen sc1\n\t" \
      "s_add_u32 %[sp], %[sp], 1\n\t" \
      "s_waitcnt vmcnt(0)\n\t" \
      "v_max3_u32 v166, v116, v117, v118\n\t" \
      "v_max_u32 v166, v166, v119\n\t" \
      "v_cmp_eq_u32 vcc, -1, v166\n\t" \
      "s_cmp_ge_u32 %[sp], 0x4000\n\t" \
      "s_cbranch_scc1 BACK20_%=\n\t" \
      "s_cbranch_vccnz LOOP20_%=\n\t" \
      "s_branch BACK20_%=\n\t" \
      "STUB21_%=:\n\t" \
      "s_mov_b32 %[sp], 0\n\t" \
      "LOOP21_%=:\n\t" \
      "s_sleep 2\n\t" \
      "buffer_load_dwordx4 v[120:123], %[tvo], %[srd], s85 offen sc1\n\t" \
      "buffer_load_dwordx4 v[124:127], %[tvo], %[srd], s86 offen sc1\n\t" \
      "buffer_load_dwordx4 v[128:131], %[tvo], %[srd], s87 offen sc1\n\t" \
      "buffer_load_dwordx4 v[132:135], %[tvo], %[srd], s88 offen sc1\n\t" \
      "s_add_u32 %[sp], %[sp], 1\n\t" \
      "s_waitcnt vmcnt(0)\n\t" \
      "v_max3_u32 v166, v120, v121, v122\n\t" \
      "v_max_u32 v166, v166, v123\n\t" \
      "v_cmp_eq_u32 vcc, -1, v166\n\t" \
      "s_cmp_ge_u32 %[sp], 0x4000\n\t" \
      "s_cbranch_scc1 BACK21_%=\n\t" \
      "s_cbranch_vccnz LOOP21_%=\n\t" \
      "s_branch BACK21_%=\n\t" \
      "STUB22_%=:\n\t" \
      "s_mov_b32 %[sp], 0\n\t" \
      "LOOP22_%=:\n\t" \
      "s_sleep 2\n\t" \
      "buffer_load_dwordx4 v[124:127], %[tvo], %[srd], s86 offen sc1\n\t" \
      "buffer_load_dwordx4 v[128:131], %[tvo], %[srd], s87 offen sc1\n\t" \
      "buffer_load_dwordx4 v[132:135], %[tvo], %[srd], s88 offen sc1\n\t" \
      "buffer_load_dwordx4 v[116:119], %[tvo], %[srd], s89 offen sc1\n\t" \
      "s_add_u32 %[sp], %[sp], 1\n\t" \
      "s_waitcnt vmcnt(0)\n\t" \
      "v_max3_u32 v166, v124, v125, v126\n\t" \
      "v_max_u32 v166, v166, v127\n\t" \
      "v_cmp_eq_u32 vcc, -1, v166\n\t" \
      "s_cmp_ge_u32 %[sp], 0x4000\n\t" \
      "s_cbranch_scc1 BACK22_%=\n\t" \
      "s_cbranch_vccnz LOOP22_%=\n\t" \
      "s_branch BACK22_%=\n\t" \
      "STUB23_%=:\n\t" \
      "s_mov_b32 %[sp], 0\n\t" \
      "LOOP23_%=:\n\t" \
      "s_sleep 2\n\t" \
      "buffer_load_dwordx4 v[128:131], %[tvo], %[srd], s87 offen sc1\n\t" \
      "buffer_load_dwordx4 v[132:135], %[tvo], %[srd], s88 offen sc1\n\t" \
      "buffer_load_dwordx4 v[116:119], %[tvo], %[srd], s89 offen sc1\n\t" \
      "buffer_load_dwordx4 v[120:123], %[tvo], %[srd], s90 offen sc1\n\t" \
      "s_add_u32 %[sp], %[sp], 1\n\t" \
      "s_waitcnt vmcnt(0)\n\t" \
      "v_max3_u32 v166, v128, v129, v130\n\t" \
      "v_max_u32 v166, v166, v131\n\t" \
      "v_cmp_eq_u32 vcc, -1, v166\n\t" \
      "s_cmp_ge_u32 %[sp], 0x4000\n\t" \
      "s_cbranch_scc1 BACK23_%=\n\t" \
      "s_cbranch_vccnz LOOP23_%=\n\t" \
      "s_branch BACK23_%=\n\t" \
      "STUB24_%=:\n\t" \
      "s_mov_b32 %[sp], 0\n\t" \
      "LOOP24_%=:\n\t" \
      "s_sleep 2\n\t" \
      "buffer_load_dwordx4 v[132:135], %[tvo], %[srd], s88 offen sc1\n\t" \
      "buffer_load_dwordx4 v[116:119], %[tvo], %[srd], s89 offen sc1\n\t" \
      "buffer_load_dwordx4 v[120:123], %[tvo], %[srd], s90 offen sc1\n\t" \
      "s_add_u32 %[sp], %[sp], 1\n\t" \
      "s_waitcnt vmcnt(0)\n\t" \
      "v_max3_u32 v166, v132, v133, v134\n\t" \
      "v_max_u32 v166, v166, v135\n\t" \
      "v_cmp_eq_u32 vcc, -1, v166\n\t" \
      "s_cmp_ge_u32 %[sp], 0x4000\n\t" \
      "s_cbranch_scc1 BACK24_%=\n\t" \
      "s_cbranch_vccnz LOOP24_%=\n\t" \
      "s_branch BACK24_%=\n\t" \
      "STUB25_%=:\n\t" \
      "s_mov_b32 %[sp], 0\n\t" \
      "LOOP25_%=:\n\t" \
      "s_sleep 2\n\t" \
      "buffer_load_dwordx4 v[116:119], %[tvo], %[srd], s89 offen sc1\n\t" \
      "buffer_load_dwordx4 v[120:123], %[tvo], %[srd], s90 offen sc1\n\t" \
      "buffer_load_dwordx4 v[124:127], %[tvo], %[srd], s91 offen sc1\n\t" \
      "s_add_u32 %[sp], %[sp], 1\n\t" \
      "s_waitcnt vmcnt(0)\n\t" \
      "v_max3_u32 v166, v116, v117, v118\n\t" \
      "v_max_u32 v166, v166, v119\n\t" \
      "v_cmp_eq_u32 vcc, -1, v166\n\t" \
      "s_cmp_ge_u32 %[sp], 0x4000\n\t" \
      "s_cbranch_scc1 BACK25_%=\n\t" \
      "s_cbranch_vccnz LOOP25_%=\n\t" \
      "s_branch BACK25_%=\n\t" \
      "STUB26_%=:\n\t" \
      "s_mov_b32 %[sp], 0\n\t" \
      "LOOP26_%=:\n\t" \
      "s_sleep 2\n\t" \
      "buffer_load_dwordx4 v[120:123], %[tvo], %[srd], s90 offen sc1\n\t" \
      "buffer_load_dwordx4 v[124:127], %[tvo], %[srd], s91 offen sc1\n\t" \
      "s_add_u32 %[sp], %[sp], 1\n\t" \
      "s_waitcnt vmcnt(0)\n\t" \
      "v_max3_u32 v166, v120, v121, v122\n\t" \
      "v_max_u32 v166, v166, v123\n\t" \
      "v_cmp_eq_u32 vcc, -1, v166\n\t" \
      "s_cmp_ge_u32 %[sp], 0x4000\n\t" \
      "s_cbranch_scc1 BACK26_%=\n\t" \
      "s_cbranch_vccnz LOOP26_%=\n\t" \
      "s_branch BACK26_%=\n\t" \
      "STUB27_%=:\n\t" \
      "s_mov_b32 %[sp], 0\n\t" \
      "LOOP27_%=:\n\t" \
      "s_sleep 2\n\t" \
      "buffer_load_dwordx4 v[124:127], %[tvo], %[srd], s91 offen sc1\n\t" \
      "s_add_u32 %[sp], %[sp], 1\n\t" \
      "s_waitcnt vmcnt(0)\n\t" \
      "v_max3_u32 v166, v124, v125, v126\n\t" \
      "v_max_u32 v166, v166, v127\n\t" \
      "v_cmp_eq_u32 vcc, -1, v166\n\t" \
      "s_cmp_ge_u32 %[sp], 0x4000\n\t" \
      "s_cbranch_scc1 BACK27_%=\n\t" \
      "s_cbranch_vccnz LOOP27_%=\n\t" \
      "s_branch BACK27_%=\n\t" \
      "STW_%=:\n\t" \
      "s_mov_b32 s92, 0x400000\n\t" \
      "STL_%=:\n\t" \
      "s_sleep 1\n\t" \
      "v_mov_b32 v167, 163828\n\t" \
      "ds_read_b32 v167, v167\n\t" \
      "s_sub_u32 s92, s92, 1\n\t" \
      "s_waitcnt lgkmcnt(0)\n\t" \
      "v_readfirstlane_b32 %[sp], v167\n\t" \
      "s_cmp_ge_u32 %[sp], 4\n\t" \
      "s_cbranch_scc1 STB_%=\n\t" \
      "s_cmp_lg_u32 s92, 0\n\t" \
      "s_cbranch_scc1 STL_%=\n\t" \
      "s_branch STB_%=\n\t" \
      "END_%=:\n\t" \
      "s_nop 1\n\t"
#define G1_CLOBBERS "v4", "v5", "v6", "v7", "v8", "v9", "v10", "v11", "v12", "v13", "v14", "v15", "v16", "v17", "v18", "v19", "v20", "v21", "v22", "v23", "v24", "v25", "v26", "v27", "v28", "v29", "v30", "v31", "v32", "v33", "v34", "v35", "v36", "v37", "v38", "v39", "v40", "v41", "v42", "v43", "v44", "v45", "v46", "v47", "v48", "v49", "v50", "v51", "v52", "v53", "v54", "v55", "v56", "v57", "v58", "v59", "v60", "v61", "v62", "v63", "v64", "v65", "v66", "v67", "v68", "v69", "v70", "v71", "v72", "v73", "v74", "v75", "v76", "v77", "v78", "v79", "v80", "v81", "v82", "v83", "v84", "v85", "v86", "v87", "v88", "v89", "v90", "v91", "v92", "v93", "v94", "v95", "v96", "v97", "v98", "v99", "v100", "v101", "v102", "v103", "v104", "v105", "v106", "v107", "v108", "v109", "v110", "v111", "v112", "v113", "v114", "v115", "v116", "v117", "v118", "v119", "v120", "v121", "v122", "v123", "v124", "v125", "v126", "v127", "v128", "v129", "v130", "v131", "v132", "v133", "v134", "v135", "v136", "v137", "v138", "v139", "v140", "v141", "v142", "v143", "v144", "v145", "v146", "v147", "v148", "v149", "v150", "v151", "v152", "v153", "v154", "v155", "v156", "v157", "v158", "v159", "v160", "v161", "v162", "v163", "v164", "v165", "v166", "v167", "s64", "s65", "s66", "s67", "s68", "s69", "s70", "s71", "s72", "s73", "s74", "s75", "s76", "s77", "s78", "s79", "s80", "s81", "s82", "s83", "s84", "s85", "s86", "s87", "s88", "s89", "s90", "s91", "s92", "vcc"
DI void gemm1_mfma(const Params& p, LAS unsigned char* lds, u32x4 srd, __amdgpu_buffer_rsrc_t rs, int rb, int cb, int tg, int rot) {
  int lane = fresh_lane();
  const int tvo = WS_H2 + rb * (224 * 512) + (4 * (lane >> 5) + 2 * ((lane >> 3) & 1) + ((lane >> 4) & 1)) * 512 + (tg * 32 + 4 * (lane & 7)) * 4;
  u32 ab = __umul24(8 * (lane >> 5) + ((lane & 15) >> 2), 448) + ((lane & 3) >> 1) * 224 + (lane & 16) + 8 * (lane & 1);
  u32 sp, hmo = WS_HM + ((rb * 4 + tg) * 112 + cb * 7) * 1024;
  asm volatile(G1_LOOP_ASM
               : [ab] "+v"(ab), [sp] "=&s"(sp), [hmo] "+s"(hmo)
               : [tvo] "v"(tvo), [srd] "s"(srd), [rot] "s"(rot * 4096), [stb] "s"(L_ST + tg * 256)
               : "memory", "scc", G1_CLOBBERS);
}

DI void refill_w1(const Params& p, int m, int lane, int cb, int rot1) {
  const u32 pc = m * 64 + lane, row = __umul24(pc, 2341) >> 16, c = pc - __umul24(row, 28);
  const unsigned char* src = (const unsigned char*)(c < 14 ? p.wg : p.wu) + (__umul24(row, FF * 2) + cb * 224 + (c < 14 ? c : c - 14) * 16);
#pragma clang loop unroll(disable)
  for (int k = 0; k < 7; ++k) {
    glds16(src + (u32)(wrap28(21 + k + rot1) * (16 * FF * 2)), k * W1_STEP + m * 1024);
    asm volatile("s_waitcnt vmcnt(1)" ::: "memory");
  }
}

DI void gemm1_side(const Params& p, LAS unsigned char* lds, __amdgpu_buffer_rsrc_t rs, u32 tag, int rb, int cb, int H, int rot1) {
  const int lane = fresh_lane();
  {
    const int t = H * 64 + lane, cr = t & 31;
    const int cvo = WS_CS + (((cr >> 3) & 1) * FF + cb * 112 + min(t >> 5, 6) * 16 + (cr >> 4) * 8 + (cr & 7)) * 8;
    u32x4 hv[2];
    u32x2 csg;
    const int vo = WS_H2 + (rb * 224 + 56 * H + (lane >> 1)) * 512 + (8 * cb + 4 * (lane & 1)) * 4;
#pragma clang loop unroll(disable)
    for (u32 spin = 0; spin < SPIN_CAP; ++spin) {
      csg = __builtin_amdgcn_raw_buffer_load_b64(rs, cvo, 0, AUX_SC1);
      hv[0] = __builtin_amdgcn_raw_buffer_load_b128(rs, vo, 0, AUX_SC1);
      hv[1] = __builtin_amdgcn_raw_buffer_load_b128(rs, lane < 48 ? vo + 32 * 512 : WS_BYTES, 0, AUX_SC1);
      const u32 m = umax3(umax3(umax3(hv[0][0], hv[0][1], hv[0][2]), hv[0][3], hv[1][0]), umax3(hv[1][1], hv[1][2], hv[1][3]), 0u);
      if (!__any(m == SENT || csg[1] != tag)) break;
      __builtin_amdgcn_s_sleep(8);
    }
    if (t < 224) *(LAS u32*)(lds + L_CS + t * 4) = csg[0];
    f32x4 a, q;
#pragma unroll
    for (int e = 0; e < 4; ++e) {
      const f32x2 u = unpack2(hv[0][e]), v = unpack2(hv[1][e]), sm = u + v, sq = u * u + v * v;
      a[e] = dpp_add<0x122>(dpp_add<0x124>(dpp_add<0x128>(sm[0] + sm[1])));
      q[e] = dpp_add<0x122>(dpp_add<0x124>(dpp_add<0x128>(sq[0] + sq[1])));
    }
    if ((lane & 15) < 2) {
      LAS unsigned char* d = lds + L_SP + ((H * 4 + (lane >> 4)) * 2 + (lane & 1)) * 32;
      *(LAS f32x4*)d = a; *(LAS f32x4*)(d + 16) = q;
    }
  }
  BARRIER();
  if (H == 0) {
    LAS unsigned char* d = lds + L_SP + ((lane >> 3) * 4 + ((lane >> 2) & 1)) * 32 + (lane & 3) * 4;
    float a = *(LAS float*)d + *(LAS float*)(d + 64), q = *(LAS float*)(d + 16) + *(LAS float*)(d + 80);
    a = dpp_add<0x128>(a); q = dpp_add<0x128>(q);
    { const auto r = __builtin_amdgcn_permlane16_swap(__float_as_uint(a), __float_as_uint(a), false, false); a = __uint_as_float(r[0]) + __uint_as_float(r[1]); }
    { const auto r = __builtin_amdgcn_permlane16_swap(__float_as_uint(q), __float_as_uint(q), false, false); q = __uint_as_float(r[0]) + __uint_as_float(r[1]); }
    a = half_sum(a) * (1.f / DM); q = half_sum(q);
    float m, sd; ln_scalars(a, fmaxf(q * (1.f / DM) - a * a, 0.f), m, sd);
    if (lane < 8) __builtin_amdgcn_raw_buffer_store_b64(u32x2{pack_bf16(m, sd), tag}, rs, WS_ST2 + (rb * 128 + 8 * cb + lane) * 8, 0, AUX_SC1);
  } else refill_w1(p, 3 + H, lane, cb, rot1);
  BARRIER();
  {
    u32x2 g;
#pragma clang loop unroll(disable)
    for (u32 spin = 0; spin < SPIN_CAP; ++spin) {
      g = __builtin_amdgcn_raw_buffer_load_b64(rs, WS_ST2 + (rb * 128 + 32 * H + col_token(lane & 31)) * 8, 0, AUX_SC1);
      if (__all(g[1] == tag)) break;
      __builtin_amdgcn_s_sleep(2);
    }
    const float ri = __builtin_amdgcn_rcpf(bf_hi(g[0]));
    if (lane < 32) *(LAS f32x2*)(lds + L_ST + (32 * H + lane) * 8) = f32x2{-bf_lo(g[0]), ri * ri};
    lds_arrive(L_CNT + 20);
  }
  VM_WAIT0();
  BARRIER();
}

DI void loader_wave(const Params& p, LAS unsigned char* lds, __amdgpu_buffer_rsrc_t rs, int L, bool conv_wg, int xcd, int idx, int rb, int cb,
                    int db, int rot1, int rot2) {
  const int lane = fresh_lane();
  const unsigned char* ds = (const unsigned char*)p.wd + L * (448 * DM * 2) + db * 128;
  const u32 dv = (lane >> 3) * (DM * 2) + (((lane & 7) ^ (((lane >> 4) & 1) << 2)) * 16);
#define FILL_WD(ks0, n, dst0) do { _Pragma("clang loop unroll(disable)") for (int k = 0; k < (n); ++k) { \
    const unsigned char* a_ = ds + wrap28((ks0) + k + rot2) * (16 * DM * 2); \
    glds16s(a_, dv, (dst0) + k * 8192 + L * 2048); \
    glds16s(a_ + 8 * DM * 2, dv, (dst0) + k * 8192 + L * 2048 + 1024); \
    asm volatile("s_waitcnt vmcnt(2)" ::: "memory"); } } while (0)
  const unsigned char* wmat = (const unsigned char*)((L & 1) ? p.wu : p.wg) + cb * 224;
  const int e0 = (L >> 1) * 64 + lane;
  u32 go[7], lo[7];
#pragma unroll
  for (int i = 0; i < 7; ++i) {
    const u32 e = e0 + 128 * i;
    const u32 r = __umul24(e, 4682) >> 16, c16 = (e - __umul24(r, 14)) * 16;
    go[i] = __umul24(r, FF * 2) + c16; lo[i] = __umul24(r, 448) + c16 + (L & 1) * 224;
    asm volatile("" : "+v"(go[i]), "+v"(lo[i]));
  }
#define GET_W1(j) (*(const GAS u32x4*)(wmat + wrap28(4 * ((j) / 7) + rot1) * (16 * FF * 2) + go[(j) % 7]))
#define PUT_W1(j, v, row0) *(LAS u32x4*)(size_t)(lo[(j) % 7] + (((j) / 7) * 64 + (row0)) * 448) = (v)
  u32x4 wr[32];
  BARRIER();
  lds_wait(L_CNT + 16, 8);
#pragma unroll
  for (int j = 0; j < 32; ++j) wr[j] = GET_W1(j);
  SB();
#pragma unroll
  for (int j = 0; j < 17; ++j) PUT_W1(j, wr[j], 0);
  SB();
#pragma unroll
  for (int j = 32; j < 37; ++j) wr[j - 32] = GET_W1(j);
  {
    const int gl = (int)blockIdx.x * 256 + L * 64 + lane;
    if (gl < 12544) glds4((const unsigned char*)p.wd + gl * 128, L_DUMMY);
  }
#define GET_WD(i0, i1) do { _Pragma("unroll") for (int i = (i0); i < (i1); ++i) \
    wr[(i + 5) % 32] = *(const GAS u32x4*)(ds + wrap28(12 + i / 2 + rot2) * (16 * DM * 2) + (i & 1) * (8 * DM * 2) + dv); SB(); } while (0)
#define PUT_WD(i0, i1, dst0) do { LAS unsigned char* d_ = opaque(lds + (dst0) + L * 2048 + lane * 16); \
    _Pragma("unroll") for (int i = (i0); i < (i1); ++i) *(LAS u32x4*)(d_ + ((i - (i0)) / 2) * 8192 + (i & 1) * 1024) = wr[(i + 5) % 32]; } while (0)
  if (conv_wg) GET_WD(0, 12);
  {
    const int l0 = (idx * 4 + L) * 98 + lane;
    glds4((const unsigned char*)p.wd + l0 * 128, L_DUMMY);
    glds4((const unsigned char*)p.wd + (l0 + (lane < 34 ? 64 : 0)) * 128, L_DUMMY);
  }
  SB();
  VM_WAIT0();
  __builtin_amdgcn_s_setprio(2);
  BARRIER();
#pragma unroll
  for (int j = 17; j < 36; ++j) PUT_W1(j, wr[j % 32], 0);
  if (e0 < 96) PUT_W1(36, wr[4], 0);
  SB();
  if (conv_wg) GET_WD(12, 32);
  BARRIER();
  refill_w1(p, L, lane, cb, rot1);
#undef PUT_W1
  SB();
  BARRIER();
  VM_WAIT0();
  if (conv_wg) FILL_WD(0, 6, WD_REG);
  BARRIER();
  if (conv_wg) FILL_WD(6, 6, 2 * WD_REG);
  VM_WAIT0();
  BARRIER();
  if (!conv_wg) return;
  BARRIER();
  PUT_WD(0, 12, 0);
  PUT_WD(12, 24, WD_REG);
  BARRIER();
  PUT_WD(24, 32, 2 * WD_REG);
  BARRIER();
  BARRIER();
#undef GET_WD
#undef PUT_WD
#undef GET_W1
#undef FILL_WD
}

DI void gemm2(const Params& p, LAS unsigned char* lds, __amdgpu_buffer_rsrc_t rs, u32 tag, int w, int rb, int cb, int rb2, int db, int rot2, bool conv_wg);

__global__ void __launch_bounds__(768, 3) fused_kernel(Params p) {
  u64 pc64;
  asm volatile("s_getpc_b64 %0" : "=s"(pc64));
  __shared__ __attribute__((aligned(16))) unsigned char lds_raw[LDS_BYTES];
  LAS unsigned char* lds = (LAS unsigned char*)lds_raw;
  const int tid = threadIdx.x, lane = tid & 63, w = __builtin_amdgcn_readfirstlane(tid >> 6);
  const int b = blockIdx.x, xcd = b & 7, idx = b >> 3;
  if (w >= 8 && idx < 3) glds4((const unsigned char*)(pc64 & ~127ull) + min(tid - 512, CODE_LINES - 1) * 128, L_DUMMY);
  SB();
  const __amdgpu_buffer_rsrc_t rs = __builtin_amdgcn_make_buffer_rsrc(p.ws, 0, WS_BYTES, 0x00020000);
  const u32x4 srd = {(u32)(size_t)p.ws, (u32)((size_t)p.ws >> 32) & 0xffffu, (u32)WS_BYTES, 0x00020000u};
  const bool conv_wg = idx < 28;
  const int pair = xcd * 28 + (conv_wg ? idx : 0);
  const int rb = 8 * (xcd >> 2) + (idx >> 2), cb = 4 * (xcd & 3) + (idx & 3);
  const int rb2 = 4 * xcd + idx / 7, db = idx % 7;
  const int rot1 = 4 * (((rb & 7) + 2 * (cb & 3)) % 7), rot2 = wrap28(4 * db + 7 * (rb2 & 3));
  const int ch = w >> 2;
  if (w >= 8) { loader_wave(p, lds, rs, w - 8, conv_wg, xcd, idx, rb, cb, db, rot1, rot2); return; }

  const int t8 = tid & 255;
  u32x4 kx0;
  u32x2 kx1;
  u32 tag, pa[7], pd;
  const int po = ((2 * pair + ch) * NST + (w & 3) * 64 + lane) * 2;
#define LOAD_PARAMS() do { \
    asm volatile("global_load_ushort %0, %6, %7\n\tglobal_load_ushort %1, %6, %8\n\tglobal_load_ushort %2, %6, %9\n\t" \
                 "global_load_ushort %3, %6, %10\n\tglobal_load_ushort %4, %6, %11\n\tglobal_load_ushort %5, %6, %12" \
                 : "=&v"(pa[0]), "=&v"(pa[1]), "=&v"(pa[2]), "=&v"(pa[3]), "=&v"(pa[4]), "=&v"(pa[5]) \
                 : "v"(po), "s"(p.are), "s"(p.aim), "s"(p.bre), "s"(p.bim), "s"(p.cre), "s"(p.cim) : "memory"); \
    asm volatile("global_load_ushort %0, %2, %3\n\tglobal_load_dword %1, %4, %5" : "=&v"(pa[6]), "=&v"(pd) \
                 : "v"((2 * pair + ch) * 2), "s"(p.ldt), "v"(pair * 4), "s"(p.dsk) : "memory"); } while (0)
#define WAIT_PARAMS(N) asm volatile("s_waitcnt vmcnt(" #N ")" : "+v"(tag), "+v"(pa[0]), "+v"(pa[1]), "+v"(pa[2]), "+v"(pa[3]), "+v"(pa[4]), "+v"(pa[5]), \
                                    "+v"(pa[6]), "+v"(pd), "+v"(kx0), "+v"(kx1) :: "memory")
  {
    u32x4 sa, xr;
    const int so = (8 * b + (lane >> 3)) * (DM * 2) + w * 112 + min(lane & 7, 6) * 16, ro = (8 * b + w) * (DM * 2) + min(lane, 55) * 16;
    asm volatile("buffer_load_dword %0, off, %3, 0 sc1\n\tglobal_load_dwordx4 %1, %4, %6\n\tglobal_load_dwordx4 %2, %5, %6"
                 : "=&v"(tag), "=&v"(sa), "=&v"(xr) : "s"(srd), "v"(so), "v"(ro), "s"(p.x) : "memory");
    if (tid < 2) *(LAS u32x4*)(lds + L_CNT + tid * 16) = u32x4{0, 0, 0, 0};
    BARRIER();
    LOAD_PARAMS();
    asm volatile("global_load_dwordx4 %0, %2, %3\n\tglobal_load_dwordx2 %1, %2, %3 offset:16" : "=&v"(kx0), "=&v"(kx1) : "v"(t8 * 16), "s"(kix_tab.v) : "memory");
    __builtin_amdgcn_raw_buffer_store_b128(u32x4{SENT, SENT, SENT, SENT}, rs, tid < 448 ? WS_H2 + b * 7168 + tid * 16 : WS_BYTES, 0, AUX_SC1);
    u32 xw = L_CV + w * PQ_WAVE + (lane >> 3) * XS_ROW + (lane & 7) * 16, xrd = L_CV + w * PQ_WAVE + (lane & 3) * (2 * XS_ROW) + (lane >> 2) * 4;
    u32 xo0 = WS_XP + (w * 28 + (lane >> 2)) * (SEQ * 8) + (8 * b + 2 * (lane & 3)) * 8, xo1 = lane < 48 ? xo0 + 16 * (SEQ * 8) : WS_BYTES;
    u32 sto = lane == 63 ? WS_STATS + (8 * b + w) * 8 : WS_BYTES, msk = lane < 56 ? 0x3F803F80u : 0u;
    asm volatile("" : "+v"(xw), "+v"(xrd), "+v"(xo0), "+v"(xo1), "+v"(sto), "+v"(msk));
    asm volatile("s_waitcnt vmcnt(11)" : "+v"(tag), "+v"(sa), "+v"(xr) :: "memory");
    tag = __builtin_amdgcn_readfirstlane(tag) + 1;
    *(LAS u32x4*)(size_t)xw = sa;
    WAVE_LDS_ORDER();
    {
      const u32 a0 = *(LAS u32*)(size_t)xrd, a1 = *(LAS u32*)(size_t)(xrd + XS_ROW), b0 = *(LAS u32*)(size_t)(xrd + 64), b1 = *(LAS u32*)(size_t)(xrd + 64 + XS_ROW);
      __builtin_amdgcn_raw_buffer_store_b128(u32x4{a0, tag, a1, tag}, rs, xo0, 0, AUX_SC1);
      __builtin_amdgcn_raw_buffer_store_b128(u32x4{b0, tag, b1, tag}, rs, xo1, 0, AUX_SC1);
    }
    float s1 = 0.f, s2 = 0.f;
#pragma unroll
    for (int e = 0; e < 4; ++e) { s1 = dot2(xr[e], msk, s1); s2 = dot2(xr[e], xr[e], s2); }
    s2 = lane < 56 ? s2 : 0.f;
    wave_sum2(s1, s2);
    s1 *= 1.f / DM;
    float m, sd; ln_scalars(s1, fmaxf(s2 * (1.f / DM) - s1 * s1, 0.f), m, sd);
    __builtin_amdgcn_raw_buffer_store_b64(u32x2{pack_bf16(m, sd), tag}, rs, sto, 0, AUX_SC1);
    lds_arrive(L_CNT + 16);
    WAIT_PARAMS(4);
  }

#define RF_POLL() do { if (w < 2) { _Pragma("clang loop unroll(disable)") for (u32 spin = 0; spin < SPIN_CAP; ++spin) { \
      const u32x4 f = __builtin_amdgcn_raw_buffer_load_b128(rs, WS_RF + tid * 16, 0, AUX_SC1); \
      if (__all(((f[0] ^ tag) | (f[1] ^ tag) | (f[2] ^ tag) | (f[3] ^ tag)) == 0)) break; \
      __builtin_amdgcn_s_sleep(2); } } } while (0)
  u32x4 g[2], x[2];
  const int gvo = w * 2048 + lane * 16;
#define LOAD_GATHER() do { _Pragma("unroll") for (int k = 0; k < 2; ++k) { \
    g[k] = __builtin_amdgcn_raw_buffer_load_b128(rs, WS_STATS + gvo + k * 1024, 0, AUX_SC1); \
    x[k] = __builtin_amdgcn_raw_buffer_load_b128(rs, WS_XP + gvo + k * 1024, pair * (SEQ * 8), AUX_SC1); } } while (0)
  if (conv_wg) {
    if (tid < 64) *(LAS u32x4*)(lds + L_U + (tid >> 5) * U_CH + (tid & 31) * 16) = u32x4{0, 0, 0, 0};
    const float dt = rbf(__expf(bf_lo(pa[6])));
    const float a = dt * bf_lo(pa[0]) * 1.44269504088896341f, bq = dt * bf_lo(pa[1]) * 0.15915494309189535f;
    const cf c0 = cmul(cf{bf_lo(pa[4]), bf_lo(pa[5])}, mk(cf{dt * bf_lo(pa[2]), dt * bf_lo(pa[3])}));
    LAS unsigned char* pq = lds + L_CV + w * PQ_WAVE;
    LAS unsigned char* pqw = opaque(pq + lane * 4);
    LAS unsigned char* pqr = opaque(pq + (lane & 31) * PQ_ROW + (lane >> 5) * 16);
#define CMULS(n, out, x, m) do { cf t_[n]; \
      _Pragma("unroll") for (int k_ = 0; k_ < (n); ++k_) t_[k_] = cf{(x)[k_][1], (x)[k_][1]} * (m)[k_].s; \
      SB(); \
      _Pragma("unroll") for (int k_ = 0; k_ < (n); ++k_) (out)[k_] = __builtin_elementwise_fma(cf{(x)[k_][0], (x)[k_][0]}, (m)[k_].z, t_[k_]); \
      SB(); } while (0)
    const cf z8 = zpow(a, bq, 8.f), z1c = zpow(a, -bq, 1.f), w1c = zpow(a, -bq, 256.f);
    {
      const cm m8 = mk(z8);
      const cf z16 = cmul(z8, m8);
      const cm m16 = mk(z16);
      cf e[4] = {cf{1.f, 0.f}, z8, z16, cmul(z16, m8)};
      const cm m32 = mk(cmul(z16, m16));
      const cm mm[4] = {m32, m32, m32, m32};
#pragma unroll
      for (int k = 0; k < 8; ++k) {
#pragma unroll
        for (int i = 0; i < 4; ++i) *(LAS u32*)(pqw + (4 * k + i) * PQ_ROW) = pack2(e[i]);
        if (k < 7) CMULS(4, e, e, mm);
      }
    }
    WAVE_LDS_ORDER();
    u32x4 qf[8];
#pragma unroll
    for (int s = 0; s < 8; ++s) qf[s] = *(LAS u32x4*)(pqr + s * 32);
    WAVE_LDS_ORDER();
    {
      const cm z1 = mk(z1c), w1 = mk(w1c);
      cf pw[8], o[7];
      pw[0] = cf{c0[0], -c0[1]};
      { const cf x[3] = {pw[0], z1c, w1c}; const cm m[3] = {z1, z1, w1}; CMULS(3, o, x, m); }
      pw[1] = o[0];
      const cm z2 = mk(o[1]), w2 = mk(o[2]);
      { const cf x[6] = {pw[0], pw[1], z2.z, w2.z, w2.z, pw[0]}; const cm m[6] = {z2, z2, z2, w1, w2, w1}; CMULS(6, o, x, m); }
      pw[2] = o[0]; pw[3] = o[1];
      const cm z4 = mk(o[2]), w3 = mk(o[3]), w4 = mk(o[4]);
#define PUT_P(row, v) *(LAS u32*)(pqw + (row) * PQ_ROW) = pack2(v)
      PUT_P(0, pw[0]); PUT_P(1, pw[1]); PUT_P(2, pw[2]); PUT_P(3, pw[3]); PUT_P(8, o[5]);
      { const cf x[7] = {pw[0], pw[1], pw[2], pw[3], pw[0], pw[2], pw[0]}; const cm m[7] = {z4, z4, z4, z4, w4, w1, w2}; CMULS(7, o, x, m); }
      pw[4] = o[0]; pw[6] = o[2];
      const cf c4 = o[4];
      PUT_P(4, o[0]); PUT_P(5, o[1]); PUT_P(6, o[2]); PUT_P(7, o[3]); PUT_P(16, o[4]); PUT_P(9, o[5]); PUT_P(12, o[6]);
      { const cf x[5] = {pw[4], pw[6], pw[4], pw[0], pw[4]}; const cm m[5] = {w1, w1, w2, w3, w3}; CMULS(5, o, x, m); }
      PUT_P(10, o[0]); PUT_P(11, o[1]); PUT_P(13, o[2]); PUT_P(14, o[3]); PUT_P(15, o[4]);
      { const cf x[4] = {c4, c4, c4, c4}; const cm m[4] = {w1, w2, w3, w4}; CMULS(4, o, x, m); }
      PUT_P(17, o[0]); PUT_P(18, o[1]); PUT_P(19, o[2]); PUT_P(20, o[3]);
#undef PUT_P
    }
#undef CMULS
    WAVE_LDS_ORDER();
    f32x16 kd;
#pragma unroll
    for (int r = 0; r < 16; ++r) kd[r] = 0.f;
#pragma unroll
    for (int s = 0; s < 8; ++s) kd = mfma32(*(LAS u32x4*)(pqr + s * 32), qf[s], kd);
    WAVE_LDS_ORDER();
    kd[11] = lane < 32 ? kd[11] : 0.f;
#pragma unroll
    for (int r = 0; r < 12; ++r) *(LAS float*)(pqw + r * 256) = kd[r];
    VM_WAIT0();
    CSYNC(ch, 4);
    if (t8 == 0) __builtin_amdgcn_raw_buffer_store_b32(tag, rs, WS_RF + (2 * b + ch) * 4, 0, AUX_SC1);
    LOAD_GATHER();
    {
      float kv[11];
#pragma unroll
      for (int e = 0; e < 11; ++e) {
        const u32 wd = e < 8 ? kx0[e >> 1] : kx1[(e - 8) >> 1];
        LAS float* t = (LAS float*)(lds + L_CV + ch * KC_CH + ((e & 1) ? wd >> 16 : wd & 0xffffu));
        kv[e] = (t[0] + t[PQ_WAVE / 4]) + (t[2 * (PQ_WAVE / 4)] + t[3 * (PQ_WAVE / 4)]);
      }
      u32 ev[5], od[5];
#pragma unroll
      for (int m = 0; m < 5; ++m) { ev[m] = pack_bf16(kv[2 * m], kv[2 * m + 1]); od[m] = pack_bf16(kv[2 * m + 1], kv[2 * m + 2]); }
      LAS unsigned char* kc = lds + L_CV + ch * KC_CH + KC_OFF + t8 * 16;
#pragma unroll
      for (int sg = 0; sg < 2; ++sg) {
        *(LAS u32x4*)(kc + (2 * sg) * KC_COPY) = u32x4{ev[sg], ev[sg + 1], ev[sg + 2], ev[sg + 3]};
        *(LAS u32x4*)(kc + (2 * sg + 1) * KC_COPY) = u32x4{od[sg], od[sg + 1], od[sg + 2], od[sg + 3]};
      }
      if (t8 < 8) *(LAS u32x4*)(lds + L_CV + ch * KC_CH + KC_OFF + (t8 >> 1) * KC_COPY + (256 + (t8 & 1)) * 16) = u32x4{0, 0, 0, 0};
    }
  } else {
    VM_WAIT0();
    CSYNC(1, 8);
    if (tid < 2) __builtin_amdgcn_raw_buffer_store_b32(tag, rs, WS_RF + (2 * b + tid) * 4, 0, AUX_SC1);
    const int j = (idx - 28) * 8 + xcd, pc = tid % 14, rg = min(tid / 14, 31);
    const u16* src = (j < 16 ? p.wg : p.wu) + (size_t)rg * FF + (j & 15) * 112 + pc * 8;
    u32x4 v[14];
#pragma unroll
    for (int k = 0; k < 14; ++k) v[k] = *(const GAS u32x4*)(src + (size_t)k * 32 * FF);
    f32x2 sm[4] = {{0.f, 0.f}, {0.f, 0.f}, {0.f, 0.f}, {0.f, 0.f}};
#pragma unroll
    for (int k = 0; k < 14; ++k)
#pragma unroll
      for (int e = 0; e < 4; ++e) sm[e] += unpack2(v[k][e]);
    if (tid < 448) {
      *(LAS f32x4*)(lds + L_CV + rg * 448 + pc * 32) = f32x4{sm[0][0], sm[0][1], sm[1][0], sm[1][1]};
      *(LAS f32x4*)(lds + L_CV + rg * 448 + pc * 32 + 16) = f32x4{sm[2][0], sm[2][1], sm[3][0], sm[3][1]};
    }
    CSYNC(0, 8);
    if (tid < 112) {
      float s = 0.f;
#pragma unroll
      for (int k = 0; k < 32; ++k) s += *(LAS float*)(lds + L_CV + k * 448 + tid * 4);
      __builtin_amdgcn_raw_buffer_store_b64(u32x2{__float_as_uint(s), tag}, rs, WS_CS + (j * 112 + tid) * 8, 0, AUX_SC1);
    }
    LOAD_GATHER();
    VM_WAIT0();
  }
  {
#pragma clang loop unroll(disable)
    for (u32 spin = 0; spin < SPIN_CAP; ++spin) {
      u32 df = 0;
#pragma unroll
      for (int k = 0; k < 2; ++k) df |= (g[k][1] ^ tag) | (g[k][3] ^ tag) | (x[k][1] ^ tag) | (x[k][3] ^ tag);
      if (__all(df == 0)) break;
      __builtin_amdgcn_s_sleep(2);
      LOAD_GATHER();
    }
    if (conv_wg) {
#pragma unroll
      for (int k = 0; k < 2; ++k) {
        u32 u[2];
#pragma unroll
        for (int e = 0; e < 2; ++e) {
          const u32 st = g[k][2 * e];
          u[e] = pack2((unpack2(x[k][2 * e]) - bf_lo(st)) * __builtin_amdgcn_rcpf(bf_hi(st)));
        }
        const int l = 256 * w + 128 * k + 2 * lane;
        *(LAS u32*)(lds + L_U + (256 + l) * 2) = __builtin_amdgcn_perm(u[1], u[0], 0x05040100u);
        *(LAS u32*)(lds + L_U + U_CH + (256 + l) * 2) = __builtin_amdgcn_perm(u[1], u[0], 0x07060302u);
        *(LAS u32x2*)(lds + L_XA + l * 4) = u32x2{x[k][0], x[k][2]};
      }
    }
    VM_WAIT0();
  }
#undef LOAD_GATHER
  if (conv_wg) {
    const u32 k0 = L_CV + ch * KC_CH + KC_OFF + (3 - (lane & 3)) * KC_COPY + (57 - 16 * (w & 3) - 2 * (lane >> 4) - ((lane & 15) >> 2)) * 8;
    CSYNC(2, 8);
    LAS unsigned char* ub = opaque(lds + L_U + ch * U_CH + (256 - 64 * (w & 3) + 16 * (lane & 15) + 8 - 8 * (lane >> 4) - 32) * 2);
    u32x4 bf[16];
#define LOAD_BF(j) do { bf[2 * (j)] = *(LAS u32x4*)(ub + 512 * (j) + 64); bf[2 * (j) + 1] = *(LAS u32x4*)(ub + 512 * (j)); } while (0)
    u32x2 ah[32];
#define LOAD_AF(c) do { _Pragma("unroll") for (int i = 0; i < 8; ++i) \
      asm volatile("ds_read_b64 %0, %2 offset:%3\n\tds_read_b64 %1, %2 offset:%4" : "=&v"(ah[(c) * 16 + 2 * i]), "=&v"(ah[(c) * 16 + 2 * i + 1]) \
                   : "v"(k0), "i"((8 - 8 * (c) + 64 * (7 - i)) * 8), "i"((9 - 8 * (c) + 64 * (7 - i)) * 8) : "memory"); } while (0)
#define PIN_AF(c, W) asm volatile(W : "+v"(ah[(c) * 16]), "+v"(ah[(c) * 16 + 1]), "+v"(ah[(c) * 16 + 2]), "+v"(ah[(c) * 16 + 3]), "+v"(ah[(c) * 16 + 4]), \
      "+v"(ah[(c) * 16 + 5]), "+v"(ah[(c) * 16 + 6]), "+v"(ah[(c) * 16 + 7]), "+v"(ah[(c) * 16 + 8]), "+v"(ah[(c) * 16 + 9]), "+v"(ah[(c) * 16 + 10]), \
      "+v"(ah[(c) * 16 + 11]), "+v"(ah[(c) * 16 + 12]), "+v"(ah[(c) * 16 + 13]), "+v"(ah[(c) * 16 + 14]), "+v"(ah[(c) * 16 + 15]), "+v"(bf[0]), "+v"(bf[1]) :: "memory")
#define AF(n) u32x4{ah[2 * (n)][0], ah[2 * (n)][1], ah[2 * (n) + 1][0], ah[2 * (n) + 1][1]}
    asm volatile("ds_read_b128 %0, %2 offset:64\n\tds_read_b128 %1, %2" : "=&v"(bf[0]), "=&v"(bf[1]) : "v"(ub) : "memory");
    LOAD_AF(0);
    PIN_AF(0, "s_waitcnt lgkmcnt(0)");
    LOAD_AF(1);
    f32x4 acc[8];
#pragma unroll
    for (int t = 0; t < 8; ++t) acc[t] = f32x4{0.f, 0.f, 0.f, 0.f};
    LAS unsigned char* pw = opaque(lds + L_CV + w * 8192 + lane * 16);
    u32 cnt = 0;
#pragma unroll
    for (int j = 0; j < 8; ++j) {
#pragma unroll
      for (int c = 0; c < 2; ++c) {
        SB();
#pragma unroll
        for (int i = 0; i + j < 8; ++i) acc[i + j] = mfma16(AF(c * 8 + i), bf[2 * j + c], acc[i + j]);
        SB();
        if (c == 0 && j == 0) {
          lds_arrive(L_CNT + 4);
          PIN_AF(1, "");
          LOAD_BF(1); LOAD_BF(2);
        }
        if (c == 0 && j == 1) cnt = *(volatile LAS u32*)(lds + L_CNT + 4);
        if (c == 0 && j == 2) { if (__builtin_amdgcn_readfirstlane(cnt) < 12) lds_wait(L_CNT + 4, 12); *(LAS f32x4*)pw = acc[0]; }
        if (c == 0 && j >= 2) *(LAS f32x4*)(pw + (j - 1) * 1024) = acc[j - 1];
      }
      if (j == 0) { LOAD_BF(3); LOAD_BF(4); }
      if (j == 1) { LOAD_BF(5); LOAD_BF(6); }
      if (j == 2) LOAD_BF(7);
    }
#undef LOAD_BF
#undef LOAD_AF
#undef PIN_AF
#undef AF
    SB();
    *(LAS f32x4*)(pw + 7 * 1024) = acc[7];
    const int l0 = 256 * w + 16 * (lane & 15) + 4 * (lane >> 4);
    const u32x4 xc = *(LAS u32x4*)(lds + L_XA + l0 * 4);
    const u32x2 ua = *(LAS u32x2*)(lds + L_U + (256 + l0) * 2), ub2 = *(LAS u32x2*)(lds + L_U + U_CH + (256 + l0) * 2);
    f32x4 y[2];
    y[0] = f32x4{bf_lo(xc[0]), bf_lo(xc[1]), bf_lo(xc[2]), bf_lo(xc[3])} + f32x4{bf_lo(ua[0]), bf_hi(ua[0]), bf_lo(ua[1]), bf_hi(ua[1])} * bf_lo(pd);
    y[1] = f32x4{bf_hi(xc[0]), bf_hi(xc[1]), bf_hi(xc[2]), bf_hi(xc[3])} + f32x4{bf_lo(ub2[0]), bf_hi(ub2[0]), bf_lo(ub2[1]), bf_hi(ub2[1])} * bf_hi(pd);
    asm volatile("" : "+v"(y[0]), "+v"(y[1]));
    RF_POLL();
    CSYNC(2, 16);
#pragma unroll
    for (int c = 0; c < 2; ++c) {
      LAS unsigned char* pp = lds + L_CV + (4 * c) * 8192 + tid * 16;
      y[c] += (*(LAS f32x4*)pp + *(LAS f32x4*)(pp + 8192)) + (*(LAS f32x4*)(pp + 2 * 8192) + *(LAS f32x4*)(pp + 3 * 8192));
    }
    u32x4 o;
#pragma unroll
    for (int e = 0; e < 4; ++e) o[e] = min(pack_bf16(y[0][e], y[1][e]), SENT - 1);
    __builtin_amdgcn_raw_buffer_store_b128(o, rs, WS_H2 + ((l0 >> 7) * 224 + pair) * 512 + (l0 & 127) * 4, 0, AUX_SC1);
  }
  if (!conv_wg) RF_POLL();
  VM_WAIT0();
  BARRIER();
  if (tid == 0 && b == 0) __builtin_amdgcn_raw_buffer_store_b32(tag, rs, 0, WS_EPOCH, AUX_SC1);

  if (w < 4) { __builtin_amdgcn_s_setprio(3); gemm1_mfma(p, lds, srd, rs, rb, cb, w, rot1); __builtin_amdgcn_s_setprio(0); }
  else { __builtin_amdgcn_s_setprio(1); gemm1_side(p, lds, rs, tag, rb, cb, w - 4, rot1); __builtin_amdgcn_s_setprio(0); }
  VM_WAIT0();
  BARRIER();
  gemm2(p, lds, rs, tag, w, rb, cb, rb2, db, rot2, conv_wg);
}

DI void gemm2(const Params& p, LAS unsigned char* lds, __amdgpu_buffer_rsrc_t rs, u32 tag, int w, int rb, int cb, int rb2, int db, int rot2, bool conv_wg) {
  int lane = fresh_lane();
  const int tid = w * 64 + lane;
  if (tid == 0) __builtin_amdgcn_raw_buffer_store_b32(tag, rs, WS_FLAG + (rb * 4 + (cb >> 2)) * 128 + (cb & 3) * 4, 0, AUX_SC1);

  if (conv_wg) {
    const int kq = w & 3, ts = w >> 2, h = lane >> 5;
    const int cgp = (lane >> 4) & 1, q = (lane & 15) >> 2, pp = lane & 3;
    LAS unsigned char* ab[2][3];
#pragma unroll
    for (int ct = 0; ct < 2; ++ct)
#pragma unroll
      for (int r = 0; r < 3; ++r)
        ab[ct][r] = opaque(lds + ((r + 1) % 3) * WD_REG + kq * 2048 + (4 * h + q) * 128 + ((ct ^ (q >> 1)) * 64) + 32 * cgp + 8 * pp);
#define WD_FRAG(s, ct) ({ LAS unsigned char* a_ = ab[ct][((s) % 18) / 6] + (((s) % 18) % 6) * 8192; const u32x2 lo_ = lds_tr(a_), hi_ = lds_tr(a_ + 1024); \
                          u32x4{lo_[0], lo_[1], hi_[0], hi_[1]}; })
    u32x4 pre[6][2];
#pragma unroll
    for (int s = 0; s < 6; ++s) { pre[s][0] = WD_FRAG(s, 0); pre[s][1] = WD_FRAG(s, 1); }
    BARRIER();
    u32 hr[4];
    const int tk = rb2 * 64 + (tid >> 8) * 32 + col_token((tid >> 3) & 31);
#pragma clang loop unroll(disable)
    for (u32 spin = 0; spin < SPIN_CAP; ++spin) {
      const u32x4 f = __builtin_amdgcn_raw_buffer_load_b128(rs, WS_FLAG + ((rb2 >> 1) * 4 + kq) * 128, 0, AUX_SC1);
      if (__all(f[0] == tag && f[1] == tag && f[2] == tag && f[3] == tag)) break;
      __builtin_amdgcn_s_sleep(1);
    }
    __builtin_amdgcn_fence(__ATOMIC_ACQUIRE, "wavefront");
    u32x4 hf[28];
    const int hvo = WS_HM + (((rb2 * 2 + ts) * 112 + kq * 28) * 64 + lane) * 16;
#pragma unroll
    for (int s = 0; s < HFW; ++s) hf[s] = __builtin_amdgcn_raw_buffer_load_b128(rs, hvo, wrap28(s + rot2) * 1024, AUX_SC1);
    SB();
    {
      const int o = WS_H2 + ((tk >> 7) * 224 + db * 32 + (tid & 7) * 4) * 512 + (tk & 127) * 4;
#pragma unroll
      for (int e = 0; e < 4; ++e) hr[e] = __builtin_amdgcn_raw_buffer_load_b32(rs, o + e * 512, 0, AUX_SC1);
    }
    SB();
    f32x16 acc[2];
#pragma unroll
    for (int ct = 0; ct < 2; ++ct)
#pragma unroll
      for (int r = 0; r < 16; ++r) acc[ct][r] = 0.f;
    u32x4 wn[2][2];
#pragma unroll
    for (int s = 0; s < 28; ++s) {
      if (s == 10 || s == 22) BARRIER();
      SB();
      if (s + 1 >= 6 && s + 1 < 28) { wn[(s + 1) & 1][0] = WD_FRAG(s + 1, 0); wn[(s + 1) & 1][1] = WD_FRAG(s + 1, 1); }
#pragma unroll
      for (int ct = 0; ct < 2; ++ct) acc[ct] = mfma32(s < 6 ? pre[s][ct] : wn[s & 1][ct], hf[s], acc[ct]);
      if (s + HFW < 28) hf[s + HFW] = __builtin_amdgcn_raw_buffer_load_b128(rs, hvo, wrap28(s + HFW + rot2) * 1024, AUX_SC1);
      SB();
    }
#undef WD_FRAG
#define PART(blk) ((blk) < 5 ? (blk) * 8704 : 2 * WD_REG + 4 * 8192 + ((blk) - 5) * 8704)
#pragma unroll
    for (int ct = 0; ct < 2; ++ct)
#pragma unroll
      for (int k = 0; k < 4; ++k)
        *(LAS f32x4*)(lds + PART(kq * 2 + ts) + (lane & 31) * 272 + (32 * ct + 8 * k + 4 * h) * 4) =
            f32x4{acc[ct][4 * k], acc[ct][4 * k + 1], acc[ct][4 * k + 2], acc[ct][4 * k + 3]};
    BARRIER();
    f32x4 r0 = {0.f, 0.f, 0.f, 0.f}, r1 = r0;
#pragma unroll
    for (int k = 0; k < 4; ++k) {
      LAS unsigned char* a = lds + PART(k * 2 + ts) + ((tid >> 3) & 31) * 272 + (tid & 7) * 32;
      r0 += *(LAS f32x4*)a; r1 += *(LAS f32x4*)(a + 16);
    }
    const u32x4 o = {pack2(unpack2(hr[0]) + unpack2(pack_bf16(r0[0], r0[1]))), pack2(unpack2(hr[1]) + unpack2(pack_bf16(r0[2], r0[3]))),
                     pack2(unpack2(hr[2]) + unpack2(pack_bf16(r1[0], r1[1]))), pack2(unpack2(hr[3]) + unpack2(pack_bf16(r1[2], r1[3])))};
    const __amdgpu_buffer_rsrc_t ro = __builtin_amdgcn_make_buffer_rsrc(p.out, 0, SEQ * DM * 2, 0x00020000);
    __builtin_amdgcn_raw_buffer_store_b128(o, ro, (tk * DM + db * 64 + (tid & 7) * 8) * 2, 0, AUX_SC1);
  }
}

extern "C" void kernel_launch(void* const* d_in, const int* in_sizes, int n_in, void* d_out, int out_size, void* d_ws, size_t ws_size,
                              hipStream_t stream) {
  Params p;
  p.x = (const u16*)d_in[0]; p.are = (const u16*)d_in[1]; p.aim = (const u16*)d_in[2]; p.bre = (const u16*)d_in[3]; p.bim = (const u16*)d_in[4];
  p.cre = (const u16*)d_in[5]; p.cim = (const u16*)d_in[6]; p.ldt = (const u16*)d_in[7]; p.dsk = (const u16*)d_in[8];
  p.wg = (const u16*)d_in[10]; p.wu = (const u16*)d_in[11]; p.wd = (const u16*)d_in[12];
  p.out = (u16*)d_out; p.ws = (unsigned char*)d_ws;
  fused_kernel<<<256, 768, 0, stream>>>(p);
}
```
